```python
import torch

def _softmax(x, axis=-1):
    return torch.softmax(x, dim=axis)

def _gelu(x):
    return torch.nn.functional.gelu(x, approximate='tanh')

def _silu(x):
    return torch.nn.functional.silu(x)

def _sigmoid(x):
    return torch.sigmoid(x)

def _layernorm(x, axis=-1, eps=1e-05):
    m = x.mean(dim=axis, keepdim=True)
    v = x.var(dim=axis, keepdim=True, unbiased=False)
    return (x - m) / torch.sqrt(v + eps)

def _scatter_add(tgt, idx, src, axis=0):
    return tgt.clone().index_add_(axis, idx, src)

def _segment_sum(src, seg, n):
    out = torch.zeros((n,) + src.shape[1:], dtype=src.dtype)
    return out.index_add_(0, seg, src)

def _pad(x, pw):
    flat = tuple((v for pair in reversed(pw) for v in pair))
    return torch.nn.functional.pad(x, flat)
kernel_name = 'standalone__tree_speculative_verify_tree_attention_over_candidate_token_tree__sampling'

def setup_inputs():

    _w = lambda a: (lambda t: t.to(torch.bfloat16) if t.is_floating_point() else t)(torch.as_tensor(a))
    B, Hq, Hkv, T, C, D = (32, 32, 4, 32, 2048, 128)
    tree_mask = torch.zeros((B, T, T), dtype=float)
    for b in range(B):
        parent = torch.zeros(T, dtype=torch.int64)
        for i in range(1, T):
            parent[i] = int(torch.randint(0, i, (1,)).item())
        for i in range(T):
            j = i
            tree_mask[b, i, j] = 1.0
            while j > 0:
                j = parent[j]
                tree_mask[b, i, j] = 1.0
    return {'q': _w((lambda *s: torch.randn(*s, dtype=torch.bfloat16) * 0.8)(B, Hq, T, D)), 'k_cache': _w((lambda *s: torch.randn(*s, dtype=torch.bfloat16) * 0.8)(B, Hkv, C, D)), 'v_cache': _w((lambda *s: torch.randn(*s, dtype=torch.bfloat16) * 0.05)(B, Hkv, C, D)), 'k_tree': _w((lambda *s: torch.randn(*s, dtype=torch.bfloat16) * 0.8)(B, Hkv, T, D)), 'v_tree': _w((lambda *s: torch.randn(*s, dtype=torch.bfloat16) * 0.05)(B, Hkv, T, D)), 'tree_mask': _w(tree_mask)}

def reference(q, k_cache, v_cache, k_tree, v_tree, tree_mask):
    B, Hq, T, D = q.shape
    Hkv, C = (k_cache.shape[1], k_cache.shape[2])
    rep = Hq // Hkv
    kc = (lambda x, n, axis: torch.repeat_interleave(x, n, dim=axis))(k_cache, rep, axis=1)
    vc = (lambda x, n, axis: torch.repeat_interleave(x, n, dim=axis))(v_cache, rep, axis=1)
    kt = (lambda x, n, axis: torch.repeat_interleave(x, n, dim=axis))(k_tree, rep, axis=1)
    vt = (lambda x, n, axis: torch.repeat_interleave(x, n, dim=axis))(v_tree, rep, axis=1)
    k = torch.concatenate([kc, kt], axis=2)
    v = torch.concatenate([vc, vt], axis=2)
    scale = D ** (-0.5)
    scores = torch.einsum('bhqd,bhkd->bhqk', q, k) * scale
    ctx_mask = torch.ones((B, 1, T, C), dtype=scores.dtype)
    tm = tree_mask[:, None, :, :].clone()
    full_mask = torch.concatenate([ctx_mask, tm], axis=-1)
    scores = scores + (1.0 - full_mask) * -1000000000.0
    p = _softmax(scores, axis=-1)
    return torch.einsum('bhqk,bhkd->bhqd', p, v)
```

```cpp
#include <hip/hip_runtime.h>
#include <hip/hip_bf16.h>
constexpr int B=32,HQ=32,HKV=4,T=32,C=2048,D=128;
__device__ __forceinline__ float bf2f(unsigned short x){return __uint_as_float(((unsigned)x)<<16);}
__global__ __launch_bounds__(128) void naive_k(const unsigned short* q,const unsigned short* kc,const unsigned short* vc,const unsigned short* kt,const unsigned short* vt,const unsigned short* tm,__hip_bfloat16* o){
  __shared__ float s[C+T]; __shared__ float qs[D]; __shared__ float red[128];
  int row=blockIdx.x; int t=row%T; int h=(row/T)%HQ; int b=row/(T*HQ); int kvh=h/(HQ/HKV); int tid=threadIdx.x;
  qs[tid]=bf2f(q[(size_t)row*D+tid]); __syncthreads();
  const unsigned short* K=kc+(size_t)(b*HKV+kvh)*C*D; const unsigned short* V=vc+(size_t)(b*HKV+kvh)*C*D;
  const unsigned short* KT=kt+(size_t)(b*HKV+kvh)*T*D; const unsigned short* VT=vt+(size_t)(b*HKV+kvh)*T*D;
  float mx=-1e30f;
  for(int k=tid;k<C+T;k+=128){ const unsigned short* kr= k<C? K+(size_t)k*D : KT+(size_t)(k-C)*D; float a=0; for(int d=0;d<D;++d) a+=qs[d]*bf2f(kr[d]); a*=0.08838834764831845f;
    if(k>=C) a+=(1.f-bf2f(tm[(b*T+t)*T+(k-C)]))*-1e9f; s[k]=a; mx=fmaxf(mx,a);}
  red[tid]=mx; __syncthreads(); for(int o2=64;o2>0;o2>>=1){ if(tid<o2) red[tid]=fmaxf(red[tid],red[tid+o2]); __syncthreads(); } mx=red[0]; __syncthreads();
  float sum=0; for(int k=tid;k<C+T;k+=128){ float e=__expf(s[k]-mx); s[k]=e; sum+=e;} red[tid]=sum; __syncthreads(); for(int o2=64;o2>0;o2>>=1){ if(tid<o2) red[tid]+=red[tid+o2]; __syncthreads(); } sum=red[0];
  float acc=0; for(int k=0;k<C;++k) acc+=s[k]*bf2f(V[(size_t)k*D+tid]); for(int k=0;k<T;++k) acc+=s[C+k]*bf2f(VT[(size_t)k*D+tid]);
  o[(size_t)row*D+tid]=__float2bfloat16(acc/sum);
}
extern "C" void kernel_launch(void* const* d_in, const int* in_sizes, int n_in, void* d_out, int out_size, void* d_ws, size_t ws_size, hipStream_t stream){
  naive_k<<<B*HQ*T,128,0,stream>>>((const unsigned short*)d_in[0],(const unsigned short*)d_in[1],(const unsigned short*)d_in[2],(const unsigned short*)d_in[3],(const unsigned short*)d_in[4],(const unsigned short*)d_in[5],(__hip_bfloat16*)d_out);
}
```

```cpp
#include <hip/hip_runtime.h>
#include <hip/hip_bf16.h>

__global__ __launch_bounds__(256, 1) void tsv_attn_kernel(const char* q, const char* kc, const char* vc, const char* kt, const char* vt, const char* tm,
                                                          char* out, char* ws) {
  __shared__ __attribute__((aligned(1024))) char lds[163840];
  unsigned tid = threadIdx.x;
  asm volatile(
    ".Ltsv_start:\n\t"
    "s_load_dwordx8 s[8:15], %[ka], 0x0\n\t"
    "s_load_dwordx8 s[16:23], %[ka], 0x20\n\t"
    "v_readfirstlane_b32 s55, %[tid]\n\t"
    "v_mbcnt_lo_u32_b32 v255, -1, 0\n\t"
    "v_mbcnt_hi_u32_b32 v255, -1, v255\n\t"
    "s_and_b32 s54, %[bid], 7\n\t"
    "s_lshl_b32 s54, s54, 4\n\t"
    "s_lshr_b32 s57, %[bid], 4\n\t"
    "s_add_u32 s54, s54, s57\n\t"
    "s_lshr_b32 s55, s55, 6\n\t"
    "v_lshrrev_b32 v96, 4, v255\n\t"
    "v_and_b32 v97, 15, v255\n\t"
    "v_lshlrev_b32 v98, 8, v96\n\t"
    "v_xor_b32 v99, v97, v96\n\t"
    "v_lshl_add_u32 v240, v99, 4, v98\n\t"
    "s_lshl_b32 s49, s55, 12\n\t"
    "s_lshl_b32 s58, s55, 13\n\t"
    "s_add_u32 m0, s58, 131072\n\t"
    "s_lshl_b32 s59, %[bid], 12\n\t"
    "s_and_b32 s59, s59, 0x8000\n\t"
    "s_lshl_b32 s57, s54, 16\n\t"
    "s_add_u32 s44, s57, s59\n\t"
    "s_lshl_b32 s42, s54, 19\n\t"
    "s_lshl_b32 s43, s54, 13\n\t"
    "s_mov_b32 s26, 32768\n\t"
    "s_mov_b32 s27, 0x20000\n\t"
    "s_mov_b32 s30, 524288\n\t"
    "s_mov_b32 s31, 0x20000\n\t"
    "s_mov_b32 s34, 524288\n\t"
    "s_mov_b32 s35, 0x20000\n\t"
    "s_mov_b32 s38, 8192\n\t"
    "s_mov_b32 s39, 0x20000\n\t"
    "s_mov_b32 s62, 8192\n\t"
    "s_mov_b32 s63, 0x20000\n\t"
    "v_lshlrev_b32 v100, 1, v96\n\t"
    "v_xor_b32 v100, v100, v97\n\t"
    "v_lshl_add_u32 v244, v100, 4, v98\n\t"
    "v_xor_b32 v241, 64, v240\n\t"
    "v_xor_b32 v242, 128, v240\n\t"
    "v_xor_b32 v243, 192, v240\n\t"
    "v_xor_b32 v245, 0x80, v244\n\t"
    "s_and_b32 s56, s55, 1\n\t"
    "s_lshl_b32 s57, s56, 7\n\t"
    "v_xor_b32 v101, s57, v240\n\t"
    "v_xor_b32 v102, s57, v241\n\t"
    "v_lshlrev_b32 v103, 6, v97\n\t"
    "v_lshl_add_u32 v103, v96, 3, v103\n\t"
    "v_mov_b32 v104, 0\n\t"
    "s_mov_b32 s40, s49\n\t"
    "s_add_u32 s41, s49, 65536\n\t"
    "s_lshl_b32 s59, s55, 11\n\t"
    "s_waitcnt lgkmcnt(0)\n\t"
    "s_add_u32 s24, s8, s44\n\t"
    "s_addc_u32 s25, s9, 0\n\t"
    "s_and_b32 s25, s25, 0xffff\n\t"
    "buffer_load_dwordx4 v240, s[24:27], s58 offen offset:0 nt lds\n\t"
    "s_mov_b32 m0, s40\n\t"
    "s_add_u32 s28, s10, s42\n\t"
    "s_addc_u32 s29, s11, 0\n\t"
    "s_and_b32 s29, s29, 0xffff\n\t"
    "buffer_load_dwordx4 v240, s[28:31], s49 offen offset:0 nt lds\n\t"
    "s_mov_b32 m0, s41\n\t"
    "s_add_u32 s32, s12, s42\n\t"
    "s_addc_u32 s33, s13, 0\n\t"
    "s_and_b32 s33, s33, 0xffff\n\t"
    "buffer_load_dwordx4 v244, s[32:35], s49 offen offset:0 nt lds\n\t"
    "s_add_u32 m0, s59, 114688\n\t"
    "s_add_u32 s36, s14, s43\n\t"
    "s_addc_u32 s37, s15, 0\n\t"
    "s_and_b32 s37, s37, 0xffff\n\t"
    "buffer_load_dwordx4 v101, s[36:39], s59 offen offset:0 nt lds\n\t"
    "s_add_u32 m0, s59, 122880\n\t"
    "s_add_u32 s60, s16, s43\n\t"
    "s_addc_u32 s61, s17, 0\n\t"
    "s_and_b32 s61, s61, 0xffff\n\t"
    "buffer_load_dwordx4 v244, s[60:63], s59 offen offset:0 nt lds\n\t"
    "s_lshr_b32 s57, s54, 2\n\t"
    "s_lshl_b32 s57, s57, 11\n\t"
    "s_add_u32 s18, s18, s57\n\t"
    "s_addc_u32 s19, s19, 0\n\t"
    "global_load_dwordx2 v[216:217], v103, s[18:19] offset:0\n\t"
    "global_load_dwordx2 v[218:219], v103, s[18:19] offset:32\n\t"
    "global_load_dwordx2 v[220:221], v103, s[18:19] offset:1024\n\t"
    "global_load_dwordx2 v[222:223], v103, s[18:19] offset:1056\n\t"
    "s_add_u32 m0, s58, 131072\n\t"
    "s_add_u32 s57, s58, 4096\n\t"
    "buffer_load_dwordx4 v241, s[24:27], s58 offen offset:1024 nt lds\n\t"
    "buffer_load_dwordx4 v242, s[24:27], s58 offen offset:2048 nt lds\n\t"
    "buffer_load_dwordx4 v243, s[24:27], s58 offen offset:3072 nt lds\n\t"
    "s_add_u32 m0, s58, 135168\n\t"
    "s_add_u32 s20, s20, s44\n\t"
    "s_addc_u32 s21, s21, 0\n\t"
    "buffer_load_dwordx4 v240, s[24:27], s57 offen offset:0 nt lds\n\t"
    "buffer_load_dwordx4 v241, s[24:27], s57 offen offset:1024 nt lds\n\t"
    "buffer_load_dwordx4 v242, s[24:27], s57 offen offset:2048 nt lds\n\t"
    "buffer_load_dwordx4 v243, s[24:27], s57 offen offset:3072 nt lds\n\t"
    "global_load_dword v105, v104, s[20:21]\n\t"
    "s_lshr_b32 s57, %[bid], 3\n\t"
    "s_lshl_b32 s57, s57, 2\n\t"
    "s_add_u32 s57, s57, s55\n\t"
    "s_lshl_b32 s57, s57, 7\n\t"
    "s_getpc_b64 s[50:51]\n\t"
    "s_min_u32 s57, s57, .Ltsv_mainend-.Ltsv_pfa\n\t"
    ".Ltsv_pfa:\n\t"
    "s_load_dword s52, s[50:51], s57\n\t"
    "s_add_u32 s57, s57, 64\n\t"
    "s_load_dword s53, s[50:51], s57\n\t"
    "s_lshl_b32 s58, s56, 13\n\t"
    "v_lshlrev_b32 v108, 8, v97\n\t"
    "v_add_u32 v108, s58, v108\n\t"
    "v_lshl_add_u32 v228, v99, 4, v108\n\t"
    "v_xor_b32 v229, 64, v228\n\t"
    "v_xor_b32 v230, 128, v228\n\t"
    "v_xor_b32 v231, 192, v228\n\t"
    "v_lshrrev_b32 v108, 2, v255\n\t"
    "v_lshlrev_b32 v109, 8, v108\n\t"
    "s_add_u32 s57, s58, 65536\n\t"
    "v_add_u32 v109, s57, v109\n\t"
    "v_and_b32 v108, 7, v108\n\t"
    "v_lshl_add_u32 v109, v108, 5, v109\n\t"
    "v_and_b32 v108, 3, v255\n\t"
    "v_lshl_add_u32 v232, v108, 3, v109\n\t"
    "v_xor_b32 v233, 32, v232\n\t"
    "v_xor_b32 v234, 64, v232\n\t"
    "v_xor_b32 v235, 96, v232\n\t"
    "v_xor_b32 v236, 128, v232\n\t"
    "v_xor_b32 v237, 160, v232\n\t"
    "v_xor_b32 v238, 192, v232\n\t"
    "v_xor_b32 v239, 224, v232\n\t"
    "v_cmp_eq_u32 vcc, 0, v108\n\t"
    "v_mov_b32 v109, 0x3f803f80\n\t"
    "s_mov_b32 s45, 0x45800000\n\t"
    "s_mov_b32 s46, 0x3e0293ee\n\t"
    "v_cndmask_b32 v224, 0, v109, vcc\n\t"
    "v_cndmask_b32 v225, 0, v109, vcc\n\t"
    "v_cndmask_b32 v226, 0, v109, vcc\n\t"
    "v_cndmask_b32 v227, 0, v109, vcc\n\t"
    "s_mov_b32 s47, 0x3e0293ee\n\t"
    "s_mov_b32 s48, 0xceabfb8f\n\t"
    "s_add_u32 s42, s49, 65536\n\t"
    "s_add_u32 s43, s49, 49152\n\t"
    "s_and_b32 s57, s55, 2\n\t"
    "s_lshl_b32 s57, s57, 13\n\t"
    "s_add_u32 s57, s57, 131072\n\t"
    "v_add_u32 v112, s57, v228\n\t"
    "v_add_u32 v113, s57, v229\n\t"
    "v_add_u32 v114, s57, v230\n\t"
    "v_add_u32 v115, s57, v231\n\t"
    "s_sub_u32 s57, 114688, s58\n\t"
    "v_add_u32 v116, s57, v228\n\t"
    "v_add_u32 v117, s57, v229\n\t"
    "v_add_u32 v118, s57, v230\n\t"
    "v_add_u32 v119, s57, v231\n\t"
    "s_add_u32 s57, s57, 4294909952\n\t"
    "v_add_u32 v120, s57, v232\n\t"
    "v_add_u32 v121, s57, v233\n\t"
    "v_add_u32 v122, s57, v234\n\t"
    "v_add_u32 v123, s57, v235\n\t"
    "v_add_u32 v124, s57, v236\n\t"
    "v_add_u32 v125, s57, v237\n\t"
    "v_add_u32 v126, s57, v238\n\t"
    "v_add_u32 v127, s57, v239\n\t"
    "s_lshl_b32 s57, s55, 13\n\t"
    "s_add_u32 s57, s57, 131072\n\t"
    "v_lshlrev_b32 v110, 4, v255\n\t"
    "v_add_u32 v110, s57, v110\n\t"
    "v_xor_b32 v111, 0x2000, v110\n\t"
    "v_mov_b32 v250, 0\n\t"
    "v_mov_b32 v251, 0\n\t"
    "v_mov_b32 v252, 0\n\t"
    "v_mov_b32 v253, 0\n\t"
    "v_mov_b32 v254, s48\n\t"
    "s_mov_b32 s44, 7\n\t"
    "s_mov_b32 s94, 24\n\t"
    "s_add_u32 m0, s40, 0\n\t"
    "s_add_u32 s57, s49, 0\n\t"
    "buffer_load_dwordx4 v241, s[28:31], s57 offen offset:1024 nt lds\n\t"
    "buffer_load_dwordx4 v242, s[28:31], s57 offen offset:2048 nt lds\n\t"
    "buffer_load_dwordx4 v243, s[28:31], s57 offen offset:3072 nt lds\n\t"
    "s_add_u32 m0, s59, 114688\n\t"
    "s_nop 0\n\t"
    "buffer_load_dwordx4 v102, s[36:39], s59 offen offset:1024 nt lds\n\t"
    "s_add_u32 m0, s40, 16384\n\t"
    "s_add_u32 s57, s49, 16384\n\t"
    "buffer_load_dwordx4 v240, s[28:31], s57 offen offset:0 nt lds\n\t"
    "s_waitcnt vmcnt(13)\n\t"
    "v_lshlrev_b32 v64, 16, v216\n\t"
    "v_fma_f32 v64, -v64, s48, v254\n\t"
    "v_and_b32 v65, 0xffff0000, v216\n\t"
    "v_fma_f32 v65, -v65, s48, v254\n\t"
    "v_lshlrev_b32 v66, 16, v217\n\t"
    "v_fma_f32 v66, -v66, s48, v254\n\t"
    "v_and_b32 v67, 0xffff0000, v217\n\t"
    "v_fma_f32 v67, -v67, s48, v254\n\t"
    "v_lshlrev_b32 v68, 16, v218\n\t"
    "v_fma_f32 v68, -v68, s48, v254\n\t"
    "v_and_b32 v69, 0xffff0000, v218\n\t"
    "v_fma_f32 v69, -v69, s48, v254\n\t"
    "v_lshlrev_b32 v70, 16, v219\n\t"
    "v_fma_f32 v70, -v70, s48, v254\n\t"
    "v_and_b32 v71, 0xffff0000, v219\n\t"
    "v_fma_f32 v71, -v71, s48, v254\n\t"
    "v_lshlrev_b32 v72, 16, v220\n\t"
    "v_fma_f32 v72, -v72, s48, v254\n\t"
    "v_and_b32 v73, 0xffff0000, v220\n\t"
    "v_fma_f32 v73, -v73, s48, v254\n\t"
    "v_lshlrev_b32 v74, 16, v221\n\t"
    "v_fma_f32 v74, -v74, s48, v254\n\t"
    "v_and_b32 v75, 0xffff0000, v221\n\t"
    "v_fma_f32 v75, -v75, s48, v254\n\t"
    "v_lshlrev_b32 v76, 16, v222\n\t"
    "v_fma_f32 v76, -v76, s48, v254\n\t"
    "v_and_b32 v77, 0xffff0000, v222\n\t"
    "v_fma_f32 v77, -v77, s48, v254\n\t"
    "v_lshlrev_b32 v78, 16, v223\n\t"
    "v_fma_f32 v78, -v78, s48, v254\n\t"
    "v_and_b32 v79, 0xffff0000, v223\n\t"
    "v_fma_f32 v79, -v79, s48, v254\n\t"
    "buffer_load_dwordx4 v241, s[28:31], s57 offen offset:1024 nt lds\n\t"
    "buffer_load_dwordx4 v242, s[28:31], s57 offen offset:2048 nt lds\n\t"
    "buffer_load_dwordx4 v243, s[28:31], s57 offen offset:3072 nt lds\n\t"
    "s_waitcnt vmcnt(13) lgkmcnt(0)\n\t"
    "ds_read_b128 v[0:3], v112\n\t"
    "ds_read_b128 v[4:7], v113\n\t"
    "ds_read_b128 v[8:11], v114\n\t"
    "ds_read_b128 v[12:15], v115\n\t"
    "s_waitcnt lgkmcnt(3)\n\t"
    "v_lshlrev_b32 v128, 16, v0\n\t"
    "v_and_b32 v129, 0xffff0000, v0\n\t"
    "v_pk_mul_f32 v[128:129], v[128:129], s[46:47]\n\t"
    "v_cvt_pk_bf16_f32 v0, v128, v129\n\t"
    "v_lshlrev_b32 v130, 16, v1\n\t"
    "v_and_b32 v131, 0xffff0000, v1\n\t"
    "v_pk_mul_f32 v[130:131], v[130:131], s[46:47]\n\t"
    "v_cvt_pk_bf16_f32 v1, v130, v131\n\t"
    "v_lshlrev_b32 v132, 16, v2\n\t"
    "v_and_b32 v133, 0xffff0000, v2\n\t"
    "v_pk_mul_f32 v[132:133], v[132:133], s[46:47]\n\t"
    "v_cvt_pk_bf16_f32 v2, v132, v133\n\t"
    "v_lshlrev_b32 v134, 16, v3\n\t"
    "v_and_b32 v135, 0xffff0000, v3\n\t"
    "v_pk_mul_f32 v[134:135], v[134:135], s[46:47]\n\t"
    "v_cvt_pk_bf16_f32 v3, v134, v135\n\t"
    "s_waitcnt lgkmcnt(2)\n\t"
    "v_lshlrev_b32 v136, 16, v4\n\t"
    "v_and_b32 v137, 0xffff0000, v4\n\t"
    "v_pk_mul_f32 v[136:137], v[136:137], s[46:47]\n\t"
    "v_cvt_pk_bf16_f32 v4, v136, v137\n\t"
    "v_lshlrev_b32 v138, 16, v5\n\t"
    "v_and_b32 v139, 0xffff0000, v5\n\t"
    "v_pk_mul_f32 v[138:139], v[138:139], s[46:47]\n\t"
    "v_cvt_pk_bf16_f32 v5, v138, v139\n\t"
    "v_lshlrev_b32 v140, 16, v6\n\t"
    "v_and_b32 v141, 0xffff0000, v6\n\t"
    "v_pk_mul_f32 v[140:141], v[140:141], s[46:47]\n\t"
    "v_cvt_pk_bf16_f32 v6, v140, v141\n\t"
    "v_lshlrev_b32 v142, 16, v7\n\t"
    "v_and_b32 v143, 0xffff0000, v7\n\t"
    "v_pk_mul_f32 v[142:143], v[142:143], s[46:47]\n\t"
    "v_cvt_pk_bf16_f32 v7, v142, v143\n\t"
    "s_waitcnt lgkmcnt(1)\n\t"
    "v_lshlrev_b32 v128, 16, v8\n\t"
    "v_and_b32 v129, 0xffff0000, v8\n\t"
    "v_pk_mul_f32 v[128:129], v[128:129], s[46:47]\n\t"
    "v_cvt_pk_bf16_f32 v8, v128, v129\n\t"
    "v_lshlrev_b32 v130, 16, v9\n\t"
    "v_and_b32 v131, 0xffff0000, v9\n\t"
    "v_pk_mul_f32 v[130:131], v[130:131], s[46:47]\n\t"
    "v_cvt_pk_bf16_f32 v9, v130, v131\n\t"
    "v_lshlrev_b32 v132, 16, v10\n\t"
    "v_and_b32 v133, 0xffff0000, v10\n\t"
    "v_pk_mul_f32 v[132:133], v[132:133], s[46:47]\n\t"
    "v_cvt_pk_bf16_f32 v10, v132, v133\n\t"
    "v_lshlrev_b32 v134, 16, v11\n\t"
    "v_and_b32 v135, 0xffff0000, v11\n\t"
    "v_pk_mul_f32 v[134:135], v[134:135], s[46:47]\n\t"
    "v_cvt_pk_bf16_f32 v11, v134, v135\n\t"
    "s_waitcnt vmcnt(9) lgkmcnt(0)\n\t"
    "ds_read_b128 v[16:19], v112 offset:4096\n\t"
    "ds_read_b128 v[20:23], v113 offset:4096\n\t"
    "ds_read_b128 v[24:27], v114 offset:4096\n\t"
    "ds_read_b128 v[28:31], v115 offset:4096\n\t"
    "s_add_u32 m0, s41, 0\n\t"
    "s_add_u32 s57, s49, 0\n\t"
    "buffer_load_dwordx4 v245, s[32:35], s57 offen offset:1024 nt lds\n\t"
    "v_lshlrev_b32 v136, 16, v12\n\t"
    "v_and_b32 v137, 0xffff0000, v12\n\t"
    "v_pk_mul_f32 v[136:137], v[136:137], s[46:47]\n\t"
    "v_cvt_pk_bf16_f32 v12, v136, v137\n\t"
    "v_lshlrev_b32 v138, 16, v13\n\t"
    "v_and_b32 v139, 0xffff0000, v13\n\t"
    "v_pk_mul_f32 v[138:139], v[138:139], s[46:47]\n\t"
    "v_cvt_pk_bf16_f32 v13, v138, v139\n\t"
    "v_lshlrev_b32 v140, 16, v14\n\t"
    "v_and_b32 v141, 0xffff0000, v14\n\t"
    "v_pk_mul_f32 v[140:141], v[140:141], s[46:47]\n\t"
    "v_cvt_pk_bf16_f32 v14, v140, v141\n\t"
    "v_lshlrev_b32 v142, 16, v15\n\t"
    "v_and_b32 v143, 0xffff0000, v15\n\t"
    "v_pk_mul_f32 v[142:143], v[142:143], s[46:47]\n\t"
    "v_cvt_pk_bf16_f32 v15, v142, v143\n\t"
    "ds_write_b128 v110, v[0:3] offset:0\n\t"
    "ds_write_b128 v110, v[4:7] offset:1024\n\t"
    "ds_write_b128 v110, v[8:11] offset:2048\n\t"
    "ds_write_b128 v110, v[12:15] offset:3072\n\t"
    "s_waitcnt lgkmcnt(7)\n\t"
    "v_lshlrev_b32 v128, 16, v16\n\t"
    "v_and_b32 v129, 0xffff0000, v16\n\t"
    "v_pk_mul_f32 v[128:129], v[128:129], s[46:47]\n\t"
    "v_cvt_pk_bf16_f32 v16, v128, v129\n\t"
    "v_lshlrev_b32 v130, 16, v17\n\t"
    "v_and_b32 v131, 0xffff0000, v17\n\t"
    "v_pk_mul_f32 v[130:131], v[130:131], s[46:47]\n\t"
    "v_cvt_pk_bf16_f32 v17, v130, v131\n\t"
    "v_lshlrev_b32 v132, 16, v18\n\t"
    "v_and_b32 v133, 0xffff0000, v18\n\t"
    "v_pk_mul_f32 v[132:133], v[132:133], s[46:47]\n\t"
    "v_cvt_pk_bf16_f32 v18, v132, v133\n\t"
    "v_lshlrev_b32 v134, 16, v19\n\t"
    "v_and_b32 v135, 0xffff0000, v19\n\t"
    "v_pk_mul_f32 v[134:135], v[134:135], s[46:47]\n\t"
    "v_cvt_pk_bf16_f32 v19, v134, v135\n\t"
    "buffer_load_dwordx4 v244, s[32:35], s57 offen offset:2048 nt lds\n\t"
    "s_waitcnt lgkmcnt(6)\n\t"
    "v_lshlrev_b32 v136, 16, v20\n\t"
    "v_and_b32 v137, 0xffff0000, v20\n\t"
    "v_pk_mul_f32 v[136:137], v[136:137], s[46:47]\n\t"
    "v_cvt_pk_bf16_f32 v20, v136, v137\n\t"
    "v_lshlrev_b32 v138, 16, v21\n\t"
    "v_and_b32 v139, 0xffff0000, v21\n\t"
    "v_pk_mul_f32 v[138:139], v[138:139], s[46:47]\n\t"
    "v_cvt_pk_bf16_f32 v21, v138, v139\n\t"
    "v_lshlrev_b32 v140, 16, v22\n\t"
    "v_and_b32 v141, 0xffff0000, v22\n\t"
    "v_pk_mul_f32 v[140:141], v[140:141], s[46:47]\n\t"
    "v_cvt_pk_bf16_f32 v22, v140, v141\n\t"
    "v_lshlrev_b32 v142, 16, v23\n\t"
    "v_and_b32 v143, 0xffff0000, v23\n\t"
    "v_pk_mul_f32 v[142:143], v[142:143], s[46:47]\n\t"
    "v_cvt_pk_bf16_f32 v23, v142, v143\n\t"
    "s_waitcnt lgkmcnt(5)\n\t"
    "v_lshlrev_b32 v128, 16, v24\n\t"
    "v_and_b32 v129, 0xffff0000, v24\n\t"
    "v_pk_mul_f32 v[128:129], v[128:129], s[46:47]\n\t"
    "v_cvt_pk_bf16_f32 v24, v128, v129\n\t"
    "v_lshlrev_b32 v130, 16, v25\n\t"
    "v_and_b32 v131, 0xffff0000, v25\n\t"
    "v_pk_mul_f32 v[130:131], v[130:131], s[46:47]\n\t"
    "v_cvt_pk_bf16_f32 v25, v130, v131\n\t"
    "v_lshlrev_b32 v132, 16, v26\n\t"
    "v_and_b32 v133, 0xffff0000, v26\n\t"
    "v_pk_mul_f32 v[132:133], v[132:133], s[46:47]\n\t"
    "v_cvt_pk_bf16_f32 v26, v132, v133\n\t"
    "v_lshlrev_b32 v134, 16, v27\n\t"
    "v_and_b32 v135, 0xffff0000, v27\n\t"
    "v_pk_mul_f32 v[134:135], v[134:135], s[46:47]\n\t"
    "v_cvt_pk_bf16_f32 v27, v134, v135\n\t"
    "buffer_load_dwordx4 v245, s[32:35], s57 offen offset:3072 nt lds\n\t"
    "s_waitcnt lgkmcnt(4)\n\t"
    "v_lshlrev_b32 v136, 16, v28\n\t"
    "v_and_b32 v137, 0xffff0000, v28\n\t"
    "v_pk_mul_f32 v[136:137], v[136:137], s[46:47]\n\t"
    "v_cvt_pk_bf16_f32 v28, v136, v137\n\t"
    "v_lshlrev_b32 v138, 16, v29\n\t"
    "v_and_b32 v139, 0xffff0000, v29\n\t"
    "v_pk_mul_f32 v[138:139], v[138:139], s[46:47]\n\t"
    "v_cvt_pk_bf16_f32 v29, v138, v139\n\t"
    "v_lshlrev_b32 v140, 16, v30\n\t"
    "v_and_b32 v141, 0xffff0000, v30\n\t"
    "v_pk_mul_f32 v[140:141], v[140:141], s[46:47]\n\t"
    "v_cvt_pk_bf16_f32 v30, v140, v141\n\t"
    "v_lshlrev_b32 v142, 16, v31\n\t"
    "v_and_b32 v143, 0xffff0000, v31\n\t"
    "v_pk_mul_f32 v[142:143], v[142:143], s[46:47]\n\t"
    "v_cvt_pk_bf16_f32 v31, v142, v143\n\t"
    "ds_write_b128 v110, v[16:19] offset:4096\n\t"
    "ds_write_b128 v110, v[20:23] offset:5120\n\t"
    "ds_write_b128 v110, v[24:27] offset:6144\n\t"
    "ds_write_b128 v110, v[28:31] offset:7168\n\t"
    "s_waitcnt vmcnt(7) lgkmcnt(0)\n\t"
    "s_barrier\n\t"
    "ds_read_b128 a[64:67], v228 offset:0\n\t"
    "ds_read_b128 a[68:71], v229 offset:0\n\t"
    "ds_read_b128 a[72:75], v230 offset:0\n\t"
    "ds_read_b128 a[76:79], v231 offset:0\n\t"
    "ds_read_b128 a[80:83], v228 offset:4096\n\t"
    "ds_read_b128 a[84:87], v229 offset:4096\n\t"
    "ds_read_b128 a[88:91], v230 offset:4096\n\t"
    "ds_read_b128 a[92:95], v231 offset:4096\n\t"
    "ds_read_b128 v[32:35], v111 offset:0\n\t"
    "ds_read_b128 v[36:39], v111 offset:1024\n\t"
    "ds_read_b128 v[40:43], v111 offset:2048\n\t"
    "ds_read_b128 v[44:47], v111 offset:3072\n\t"
    "s_add_u32 m0, s59, 122880\n\t"
    "s_nop 0\n\t"
    "buffer_load_dwordx4 v245, s[60:63], s59 offen offset:1024 nt lds\n\t"
    "s_waitcnt lgkmcnt(8)\n\t"
    "v_mfma_f32_16x16x32_bf16 v[128:131], a[64:67], v[0:3], 0\n\t"
    "ds_read_b128 v[48:51], v111 offset:4096\n\t"
    "v_mfma_f32_16x16x32_bf16 v[128:131], a[68:71], v[4:7], v[128:131]\n\t"
    "ds_read_b128 v[52:55], v111 offset:5120\n\t"
    "v_mfma_f32_16x16x32_bf16 v[128:131], a[72:75], v[8:11], v[128:131]\n\t"
    "ds_read_b128 v[56:59], v111 offset:6144\n\t"
    "v_mfma_f32_16x16x32_bf16 v[128:131], a[76:79], v[12:15], v[128:131]\n\t"
    "ds_read_b128 v[60:63], v111 offset:7168\n\t"
    "v_mfma_f32_16x16x32_bf16 v[136:139], a[64:67], v[16:19], 0\n\t"
    "ds_read_b128 a[96:99], v116 offset:0\n\t"
    "v_mfma_f32_16x16x32_bf16 v[136:139], a[68:71], v[20:23], v[136:139]\n\t"
    "v_mfma_f32_16x16x32_bf16 v[136:139], a[72:75], v[24:27], v[136:139]\n\t"
    "v_mfma_f32_16x16x32_bf16 v[136:139], a[76:79], v[28:31], v[136:139]\n\t"
    "s_waitcnt lgkmcnt(9)\n\t"
    "v_mfma_f32_16x16x32_bf16 v[132:135], a[80:83], v[0:3], 0\n\t"
    "ds_read_b128 a[100:103], v117 offset:0\n\t"
    "v_mfma_f32_16x16x32_bf16 v[132:135], a[84:87], v[4:7], v[132:135]\n\t"
    "s_add_u32 m0, s40, 32768\n\t"
    "s_add_u32 s57, s49, 32768\n\t"
    "buffer_load_dwordx4 v240, s[28:31], s57 offen offset:0 nt lds\n\t"
    "v_mfma_f32_16x16x32_bf16 v[132:135], a[88:91], v[8:11], v[132:135]\n\t"
    "ds_read_b128 a[104:107], v118 offset:0\n\t"
    "v_mfma_f32_16x16x32_bf16 v[132:135], a[92:95], v[12:15], v[132:135]\n\t"
    "ds_read_b128 a[108:111], v119 offset:0\n\t"
    "v_mfma_f32_16x16x32_bf16 v[140:143], a[80:83], v[16:19], 0\n\t"
    "ds_read_b128 a[112:115], v116 offset:4096\n\t"
    "v_mfma_f32_16x16x32_bf16 v[140:143], a[84:87], v[20:23], v[140:143]\n\t"
    "ds_read_b128 a[116:119], v117 offset:4096\n\t"
    "v_mfma_f32_16x16x32_bf16 v[140:143], a[88:91], v[24:27], v[140:143]\n\t"
    "ds_read_b128 a[120:123], v118 offset:4096\n\t"
    "v_mfma_f32_16x16x32_bf16 v[140:143], a[92:95], v[28:31], v[140:143]\n\t"
    "s_waitcnt lgkmcnt(10)\n\t"
    "ds_read_b128 a[124:127], v119 offset:4096\n\t"
    "s_waitcnt lgkmcnt(8)\n\t"
    "v_mfma_f32_16x16x32_bf16 v[144:147], a[64:67], v[32:35], 0\n\t"
    "s_nop 3\n\t"
    "v_max3_f32 v246, v128, v129, v130\n\t"
    "v_max3_f32 v246, v246, v131, v132\n\t"
    "v_mfma_f32_16x16x32_bf16 v[144:147], a[68:71], v[36:39], v[144:147]\n\t"
    "v_max3_f32 v246, v246, v133, v134\n\t"
    "v_max_f32 v246, v246, v135\n\t"
    "v_mfma_f32_16x16x32_bf16 v[144:147], a[72:75], v[40:43], v[144:147]\n\t"
    "v_max3_f32 v247, v136, v137, v138\n\t"
    "v_max3_f32 v247, v247, v139, v140\n\t"
    "v_mfma_f32_16x16x32_bf16 v[144:147], a[76:79], v[44:47], v[144:147]\n\t"
    "v_max3_f32 v247, v247, v141, v142\n\t"
    "v_max_f32 v247, v247, v143\n\t"
    "v_mfma_f32_16x16x32_bf16 v[152:155], a[64:67], v[48:51], 0\n\t"
    "s_nop 1\n\t"
    "v_permlane32_swap_b32 v246, v247\n\t"
    "v_max_f32 v246, v246, v247\n\t"
    "v_mfma_f32_16x16x32_bf16 v[152:155], a[68:71], v[52:55], v[152:155]\n\t"
    "v_mov_b32 v247, v246\n\t"
    "s_nop 1\n\t"
    "v_permlane16_swap_b32 v246, v247\n\t"
    "v_mfma_f32_16x16x32_bf16 v[152:155], a[72:75], v[56:59], v[152:155]\n\t"
    "v_max_f32 v246, v246, v247\n\t"
    "v_mov_b32 v247, v246\n\t"
    "v_mfma_f32_16x16x32_bf16 v[152:155], a[76:79], v[60:63], v[152:155]\n\t"
    "buffer_load_dwordx4 v241, s[28:31], s57 offen offset:1024 nt lds\n\t"
    "s_nop 1\n\t"
    "v_permlane32_swap_b32 v246, v247\n\t"
    "v_sub_f32 v128, v128, v246\n\t"
    "v_mfma_f32_16x16x32_bf16 v[148:151], a[80:83], v[32:35], 0\n\t"
    "v_sub_f32 v129, v129, v246\n\t"
    "v_sub_f32 v130, v130, v246\n\t"
    "v_mfma_f32_16x16x32_bf16 v[148:151], a[84:87], v[36:39], v[148:151]\n\t"
    "v_sub_f32 v131, v131, v246\n\t"
    "v_sub_f32 v132, v132, v246\n\t"
    "v_mfma_f32_16x16x32_bf16 v[148:151], a[88:91], v[40:43], v[148:151]\n\t"
    "v_sub_f32 v133, v133, v246\n\t"
    "v_sub_f32 v134, v134, v246\n\t"
    "v_mfma_f32_16x16x32_bf16 v[148:151], a[92:95], v[44:47], v[148:151]\n\t"
    "v_sub_f32 v135, v135, v246\n\t"
    "v_sub_f32 v136, v136, v247\n\t"
    "v_mfma_f32_16x16x32_bf16 v[156:159], a[80:83], v[48:51], 0\n\t"
    "v_sub_f32 v137, v137, v247\n\t"
    "v_sub_f32 v138, v138, v247\n\t"
    "v_mfma_f32_16x16x32_bf16 v[156:159], a[84:87], v[52:55], v[156:159]\n\t"
    "v_sub_f32 v139, v139, v247\n\t"
    "v_sub_f32 v140, v140, v247\n\t"
    "v_mfma_f32_16x16x32_bf16 v[156:159], a[88:91], v[56:59], v[156:159]\n\t"
    "v_sub_f32 v141, v141, v247\n\t"
    "v_sub_f32 v142, v142, v247\n\t"
    "v_mfma_f32_16x16x32_bf16 v[156:159], a[92:95], v[60:63], v[156:159]\n\t"
    "v_sub_f32 v143, v143, v247\n\t"
    "s_waitcnt vmcnt(6) lgkmcnt(0)\n\t"
    "s_barrier\n\t"
    "v_mfma_f32_16x16x32_bf16 v[64:67], a[96:99], v[32:35], v[64:67]\n\t"
    "ds_read_b128 a[64:67], v228 offset:16384\n\t"
    "v_exp_f32 v128, v128\n\t"
    "v_exp_f32 v129, v129\n\t"
    "v_mfma_f32_16x16x32_bf16 v[64:67], a[100:103], v[36:39], v[64:67]\n\t"
    "v_exp_f32 v130, v130\n\t"
    "v_mfma_f32_16x16x32_bf16 v[64:67], a[104:107], v[40:43], v[64:67]\n\t"
    "ds_read_b128 a[68:71], v229 offset:16384\n\t"
    "v_exp_f32 v131, v131\n\t"
    "v_exp_f32 v132, v132\n\t"
    "v_mfma_f32_16x16x32_bf16 v[64:67], a[108:111], v[44:47], v[64:67]\n\t"
    "v_exp_f32 v133, v133\n\t"
    "v_mfma_f32_16x16x32_bf16 v[72:75], a[96:99], v[48:51], v[72:75]\n\t"
    "ds_read_b128 a[72:75], v230 offset:16384\n\t"
    "v_exp_f32 v134, v134\n\t"
    "v_exp_f32 v135, v135\n\t"
    "v_mfma_f32_16x16x32_bf16 v[72:75], a[100:103], v[52:55], v[72:75]\n\t"
    "buffer_load_dwordx4 v242, s[28:31], s57 offen offset:2048 nt lds\n\t"
    "v_exp_f32 v136, v136\n\t"
    "v_mfma_f32_16x16x32_bf16 v[72:75], a[104:107], v[56:59], v[72:75]\n\t"
    "ds_read_b128 a[76:79], v231 offset:16384\n\t"
    "v_exp_f32 v137, v137\n\t"
    "v_exp_f32 v138, v138\n\t"
    "v_mfma_f32_16x16x32_bf16 v[72:75], a[108:111], v[60:63], v[72:75]\n\t"
    "v_exp_f32 v139, v139\n\t"
    "v_mfma_f32_16x16x32_bf16 v[68:71], a[112:115], v[32:35], v[68:71]\n\t"
    "ds_read_b128 a[80:83], v228 offset:20480\n\t"
    "v_exp_f32 v140, v140\n\t"
    "v_mfma_f32_16x16x32_bf16 v[68:71], a[116:119], v[36:39], v[68:71]\n\t"
    "v_exp_f32 v141, v141\n\t"
    "v_exp_f32 v142, v142\n\t"
    "v_mfma_f32_16x16x32_bf16 v[68:71], a[120:123], v[40:43], v[68:71]\n\t"
    "ds_read_b128 a[84:87], v229 offset:20480\n\t"
    "v_exp_f32 v143, v143\n\t"
    "v_mfma_f32_16x16x32_bf16 v[68:71], a[124:127], v[44:47], v[68:71]\n\t"
    "v_sub_f32 v160, 0, v246\n\t"
    "v_sub_f32 v161, 0, v246\n\t"
    "v_mov_b64 v[162:163], v[160:161]\n\t"
    "v_mfma_f32_16x16x32_bf16 v[76:79], a[112:115], v[48:51], v[76:79]\n\t"
    "ds_read_b128 a[88:91], v230 offset:20480\n\t"
    "v_sub_f32 v164, 0, v247\n\t"
    "v_sub_f32 v165, 0, v247\n\t"
    "v_mov_b64 v[166:167], v[164:165]\n\t"
    "v_mfma_f32_16x16x32_bf16 v[76:79], a[116:119], v[52:55], v[76:79]\n\t"
    "buffer_load_dwordx4 v243, s[28:31], s57 offen offset:3072 nt lds\n\t"
    "v_cvt_pk_bf16_f32 v176, v128, v129\n\t"
    "v_cvt_pk_bf16_f32 v177, v130, v131\n\t"
    "v_cvt_pk_bf16_f32 v178, v132, v133\n\t"
    "v_mfma_f32_16x16x32_bf16 v[76:79], a[120:123], v[56:59], v[76:79]\n\t"
    "ds_read_b128 a[92:95], v231 offset:20480\n\t"
    "v_cvt_pk_bf16_f32 v179, v134, v135\n\t"
    "v_cvt_pk_bf16_f32 v180, v136, v137\n\t"
    "v_cvt_pk_bf16_f32 v181, v138, v139\n\t"
    "v_mfma_f32_16x16x32_bf16 v[76:79], a[124:127], v[60:63], v[76:79]\n\t"
    "v_cvt_pk_bf16_f32 v182, v140, v141\n\t"
    "v_cvt_pk_bf16_f32 v183, v142, v143\n\t"
    "s_waitcnt vmcnt(4) lgkmcnt(0)\n\t"
    "s_barrier\n\t"
    "v_mfma_f32_16x16x32_bf16 v[128:131], a[64:67], v[0:3], v[160:163]\n\t"
    "ds_read_b64_tr_b16 a[96:97], v232 offset:0\n\t"
    "v_max3_f32 v110, v144, v145, v146\n\t"
    "v_max3_f32 v110, v110, v147, v148\n\t"
    "v_max3_f32 v110, v110, v149, v150\n\t"
    "v_mfma_f32_16x16x32_bf16 v[128:131], a[68:71], v[4:7], v[128:131]\n\t"
    "ds_read_b64_tr_b16 a[98:99], v232 offset:4096\n\t"
    "v_max3_f32 v110, v110, v151, v64\n\t"
    "v_max3_f32 v110, v110, v65, v66\n\t"
    "v_mfma_f32_16x16x32_bf16 v[208:211], v[224:227], v[176:179], 0\n\t"
    "ds_read_b64_tr_b16 a[100:101], v233 offset:0\n\t"
    "v_max3_f32 v110, v110, v67, v68\n\t"
    "v_max3_f32 v110, v110, v69, v70\n\t"
    "v_mfma_f32_16x16x32_bf16 v[212:215], v[224:227], v[180:183], 0\n\t"
    "ds_read_b64_tr_b16 a[102:103], v233 offset:4096\n\t"
    "v_max_f32 v110, v110, v71\n\t"
    "v_max3_f32 v111, v152, v153, v154\n\t"
    "v_mfma_f32_16x16x32_bf16 v[128:131], a[72:75], v[8:11], v[128:131]\n\t"
    "ds_read_b64_tr_b16 a[104:105], v234 offset:0\n\t"
    "v_max3_f32 v111, v111, v155, v156\n\t"
    "v_max3_f32 v111, v111, v157, v158\n\t"
    "v_mfma_f32_16x16x32_bf16 v[128:131], a[76:79], v[12:15], v[128:131]\n\t"
    "ds_read_b64_tr_b16 a[106:107], v234 offset:4096\n\t"
    "v_max3_f32 v111, v111, v159, v72\n\t"
    "v_max3_f32 v111, v111, v73, v74\n\t"
    "v_mfma_f32_16x16x32_bf16 v[136:139], a[64:67], v[16:19], v[164:167]\n\t"
    "ds_read_b64_tr_b16 a[108:109], v235 offset:0\n\t"
    "v_max3_f32 v111, v111, v75, v76\n\t"
    "v_max3_f32 v111, v111, v77, v78\n\t"
    "v_max_f32 v111, v111, v79\n\t"
    "v_mfma_f32_16x16x32_bf16 v[136:139], a[68:71], v[20:23], v[136:139]\n\t"
    "ds_read_b64_tr_b16 a[110:111], v235 offset:4096\n\t"
    "s_nop 1\n\t"
    "v_permlane32_swap_b32 v110, v111\n\t"
    "v_max_f32 v110, v110, v111\n\t"
    "v_mfma_f32_16x16x32_bf16 v[136:139], a[72:75], v[24:27], v[136:139]\n\t"
    "ds_read_b64_tr_b16 a[112:113], v236 offset:0\n\t"
    "v_mov_b32 v111, v110\n\t"
    "s_nop 1\n\t"
    "v_permlane16_swap_b32 v110, v111\n\t"
    "v_mfma_f32_16x16x32_bf16 v[136:139], a[76:79], v[28:31], v[136:139]\n\t"
    "ds_read_b64_tr_b16 a[114:115], v236 offset:4096\n\t"
    "v_max_f32 v110, v110, v111\n\t"
    "v_mov_b32 v111, v110\n\t"
    "v_mfma_f32_16x16x32_bf16 v[132:135], a[80:83], v[0:3], v[160:163]\n\t"
    "ds_read_b64_tr_b16 a[116:117], v237 offset:0\n\t"
    "s_nop 1\n\t"
    "v_permlane32_swap_b32 v110, v111\n\t"
    "v_sub_f32 v64, v64, v110\n\t"
    "v_mfma_f32_16x16x32_bf16 v[132:135], a[84:87], v[4:7], v[132:135]\n\t"
    "ds_read_b64_tr_b16 a[118:119], v237 offset:4096\n\t"
    "v_sub_f32 v65, v65, v110\n\t"
    "v_sub_f32 v66, v66, v110\n\t"
    "v_mfma_f32_16x16x32_bf16 v[132:135], a[88:91], v[8:11], v[132:135]\n\t"
    "ds_read_b64_tr_b16 a[120:121], v238 offset:0\n\t"
    "v_sub_f32 v67, v67, v110\n\t"
    "v_sub_f32 v68, v68, v110\n\t"
    "v_sub_f32 v69, v69, v110\n\t"
    "v_mfma_f32_16x16x32_bf16 v[132:135], a[92:95], v[12:15], v[132:135]\n\t"
    "ds_read_b64_tr_b16 a[122:123], v238 offset:4096\n\t"
    "v_sub_f32 v70, v70, v110\n\t"
    "v_sub_f32 v71, v71, v110\n\t"
    "v_mfma_f32_16x16x32_bf16 v[140:143], a[80:83], v[16:19], v[164:167]\n\t"
    "ds_read_b64_tr_b16 a[124:125], v239 offset:0\n\t"
    "v_sub_f32 v72, v72, v111\n\t"
    "v_sub_f32 v73, v73, v111\n\t"
    "v_mfma_f32_16x16x32_bf16 v[140:143], a[84:87], v[20:23], v[140:143]\n\t"
    "s_waitcnt lgkmcnt(10)\n\t"
    "ds_read_b64_tr_b16 a[126:127], v239 offset:4096\n\t"
    "v_sub_f32 v74, v74, v111\n\t"
    "v_sub_f32 v75, v75, v111\n\t"
    "v_mfma_f32_16x16x32_bf16 v[140:143], a[88:91], v[24:27], v[140:143]\n\t"
    "s_add_u32 m0, s41, 16384\n\t"
    "s_add_u32 s57, s49, 16384\n\t"
    "buffer_load_dwordx4 v244, s[32:35], s57 offen offset:0 nt lds\n\t"
    "v_sub_f32 v76, v76, v111\n\t"
    "v_sub_f32 v77, v77, v111\n\t"
    "v_mfma_f32_16x16x32_bf16 v[140:143], a[92:95], v[28:31], v[140:143]\n\t"
    "buffer_load_dwordx4 v245, s[32:35], s57 offen offset:1024 nt lds\n\t"
    "v_sub_f32 v78, v78, v111\n\t"
    "v_sub_f32 v79, v79, v111\n\t"
    "s_waitcnt lgkmcnt(11)\n\t"
    "v_mfma_f32_16x16x32_bf16 a[0:3], a[96:99], v[176:179], 0\n\t"
    "ds_read_b64_tr_b16 a[128:129], v120 offset:0\n\t"
    "v_exp_f32 v64, v64\n\t"
    "v_exp_f32 v65, v65\n\t"
    "v_mfma_f32_16x16x32_bf16 a[4:7], a[96:99], v[180:183], 0\n\t"
    "ds_read_b64_tr_b16 a[130:131], v120 offset:4096\n\t"
    "v_exp_f32 v66, v66\n\t"
    "v_exp_f32 v67, v67\n\t"
    "v_mfma_f32_16x16x32_bf16 a[8:11], a[100:103], v[176:179], 0\n\t"
    "ds_read_b64_tr_b16 a[132:133], v121 offset:0\n\t"
    "v_exp_f32 v68, v68\n\t"
    "v_exp_f32 v69, v69\n\t"
    "v_mfma_f32_16x16x32_bf16 a[12:15], a[100:103], v[180:183], 0\n\t"
    "ds_read_b64_tr_b16 a[134:135], v121 offset:4096\n\t"
    "v_exp_f32 v70, v70\n\t"
    "v_exp_f32 v71, v71\n\t"
    "s_waitcnt lgkmcnt(12)\n\t"
    "v_mfma_f32_16x16x32_bf16 a[16:19], a[104:107], v[176:179], 0\n\t"
    "ds_read_b64_tr_b16 a[136:137], v122 offset:0\n\t"
    "v_exp_f32 v72, v72\n\t"
    "v_exp_f32 v73, v73\n\t"
    "v_mfma_f32_16x16x32_bf16 a[20:23], a[104:107], v[180:183], 0\n\t"
    "ds_read_b64_tr_b16 a[138:139], v122 offset:4096\n\t"
    "v_exp_f32 v74, v74\n\t"
    "v_exp_f32 v75, v75\n\t"
    "v_mfma_f32_16x16x32_bf16 a[24:27], a[108:111], v[176:179], 0\n\t"
    "ds_read_b64_tr_b16 a[140:141], v123 offset:0\n\t"
    "v_exp_f32 v76, v76\n\t"
    "v_exp_f32 v77, v77\n\t"
    "v_mfma_f32_16x16x32_bf16 a[28:31], a[108:111], v[180:183], 0\n\t"
    "s_waitcnt lgkmcnt(10)\n\t"
    "ds_read_b64_tr_b16 a[142:143], v123 offset:4096\n\t"
    "v_exp_f32 v78, v78\n\t"
    "v_exp_f32 v79, v79\n\t"
    "s_waitcnt lgkmcnt(11)\n\t"
    "v_mfma_f32_16x16x32_bf16 a[32:35], a[112:115], v[176:179], 0\n\t"
    "ds_read_b64_tr_b16 a[144:145], v124 offset:0\n\t"
    "v_sub_f32 v168, 0, v110\n\t"
    "v_sub_f32 v169, 0, v110\n\t"
    "v_mov_b64 v[170:171], v[168:169]\n\t"
    "v_mfma_f32_16x16x32_bf16 a[36:39], a[112:115], v[180:183], 0\n\t"
    "ds_read_b64_tr_b16 a[146:147], v124 offset:4096\n\t"
    "v_sub_f32 v172, 0, v111\n\t"
    "v_sub_f32 v173, 0, v111\n\t"
    "v_mov_b64 v[174:175], v[172:173]\n\t"
    "v_cvt_pk_bf16_f32 v200, v64, v65\n\t"
    "v_mfma_f32_16x16x32_bf16 a[40:43], a[116:119], v[176:179], 0\n\t"
    "ds_read_b64_tr_b16 a[148:149], v125 offset:0\n\t"
    "v_cvt_pk_bf16_f32 v201, v66, v67\n\t"
    "v_cvt_pk_bf16_f32 v202, v68, v69\n\t"
    "v_cvt_pk_bf16_f32 v203, v70, v71\n\t"
    "v_cvt_pk_bf16_f32 v204, v72, v73\n\t"
    "v_mfma_f32_16x16x32_bf16 a[44:47], a[116:119], v[180:183], 0\n\t"
    "ds_read_b64_tr_b16 a[150:151], v125 offset:4096\n\t"
    "v_cvt_pk_bf16_f32 v205, v74, v75\n\t"
    "v_cvt_pk_bf16_f32 v206, v76, v77\n\t"
    "v_cvt_pk_bf16_f32 v207, v78, v79\n\t"
    "v_sub_f32 v144, v144, v110\n\t"
    "s_waitcnt lgkmcnt(12)\n\t"
    "v_mfma_f32_16x16x32_bf16 a[48:51], a[120:123], v[176:179], 0\n\t"
    "ds_read_b64_tr_b16 a[152:153], v126 offset:0\n\t"
    "v_sub_f32 v145, v145, v110\n\t"
    "v_sub_f32 v146, v146, v110\n\t"
    "v_sub_f32 v147, v147, v110\n\t"
    "v_sub_f32 v148, v148, v110\n\t"
    "v_mfma_f32_16x16x32_bf16 a[52:55], a[120:123], v[180:183], 0\n\t"
    "ds_read_b64_tr_b16 a[154:155], v126 offset:4096\n\t"
    "v_sub_f32 v149, v149, v110\n\t"
    "v_sub_f32 v150, v150, v110\n\t"
    "v_sub_f32 v151, v151, v110\n\t"
    "v_sub_f32 v152, v152, v111\n\t"
    "v_mfma_f32_16x16x32_bf16 a[56:59], a[124:127], v[176:179], 0\n\t"
    "ds_read_b64_tr_b16 a[156:157], v127 offset:0\n\t"
    "v_sub_f32 v153, v153, v111\n\t"
    "v_sub_f32 v154, v154, v111\n\t"
    "v_sub_f32 v155, v155, v111\n\t"
    "v_sub_f32 v156, v156, v111\n\t"
    "v_mfma_f32_16x16x32_bf16 a[60:63], a[124:127], v[180:183], 0\n\t"
    "s_waitcnt lgkmcnt(10)\n\t"
    "ds_read_b64_tr_b16 a[158:159], v127 offset:4096\n\t"
    "v_sub_f32 v157, v157, v111\n\t"
    "v_sub_f32 v158, v158, v111\n\t"
    "v_sub_f32 v159, v159, v111\n\t"
    "buffer_load_dwordx4 v244, s[32:35], s57 offen offset:2048 nt lds\n\t"
    "buffer_load_dwordx4 v245, s[32:35], s57 offen offset:3072 nt lds\n\t"
    "s_waitcnt lgkmcnt(0)\n\t"
    "v_mfma_f32_16x16x32_bf16 v[216:219], v[224:227], v[200:203], 0\n\t"
    "v_exp_f32 v144, v144\n\t"
    "v_exp_f32 v145, v145\n\t"
    "v_mfma_f32_16x16x32_bf16 v[220:223], v[224:227], v[204:207], 0\n\t"
    "v_exp_f32 v146, v146\n\t"
    "v_exp_f32 v147, v147\n\t"
    "v_mfma_f32_16x16x32_bf16 v[64:67], a[128:131], v[200:203], 0\n\t"
    "v_exp_f32 v148, v148\n\t"
    "v_exp_f32 v149, v149\n\t"
    "v_mfma_f32_16x16x32_bf16 v[68:71], a[128:131], v[204:207], 0\n\t"
    "s_add_u32 m0, s40, 49152\n\t"
    "s_add_u32 s57, s49, 49152\n\t"
    "buffer_load_dwordx4 v240, s[28:31], s57 offen offset:0 nt lds\n\t"
    "v_exp_f32 v150, v150\n\t"
    "v_exp_f32 v151, v151\n\t"
    "v_mfma_f32_16x16x32_bf16 v[72:75], a[132:135], v[200:203], 0\n\t"
    "v_exp_f32 v152, v152\n\t"
    "v_exp_f32 v153, v153\n\t"
    "v_mfma_f32_16x16x32_bf16 v[76:79], a[132:135], v[204:207], 0\n\t"
    "v_exp_f32 v154, v154\n\t"
    "v_exp_f32 v155, v155\n\t"
    "v_mfma_f32_16x16x32_bf16 v[80:83], a[136:139], v[200:203], 0\n\t"
    "v_exp_f32 v156, v156\n\t"
    "v_exp_f32 v157, v157\n\t"
    "v_mfma_f32_16x16x32_bf16 v[84:87], a[136:139], v[204:207], 0\n\t"
    "buffer_load_dwordx4 v241, s[28:31], s57 offen offset:1024 nt lds\n\t"
    "v_exp_f32 v158, v158\n\t"
    "v_mfma_f32_16x16x32_bf16 v[88:91], a[140:143], v[200:203], 0\n\t"
    "v_exp_f32 v159, v159\n\t"
    "v_add_f32 v250, v250, v208\n\t"
    "v_add_f32 v251, v251, v212\n\t"
    "v_mfma_f32_16x16x32_bf16 v[92:95], a[140:143], v[204:207], 0\n\t"
    "v_cvt_pk_bf16_f32 v184, v144, v145\n\t"
    "v_cvt_pk_bf16_f32 v185, v146, v147\n\t"
    "v_cvt_pk_bf16_f32 v186, v148, v149\n\t"
    "v_cvt_pk_bf16_f32 v187, v150, v151\n\t"
    "v_mfma_f32_16x16x32_bf16 v[96:99], a[144:147], v[200:203], 0\n\t"
    "v_cvt_pk_bf16_f32 v188, v152, v153\n\t"
    "v_cvt_pk_bf16_f32 v189, v154, v155\n\t"
    "v_cvt_pk_bf16_f32 v190, v156, v157\n\t"
    "v_mfma_f32_16x16x32_bf16 v[100:103], a[144:147], v[204:207], 0\n\t"
    "buffer_load_dwordx4 v242, s[28:31], s57 offen offset:2048 nt lds\n\t"
    "v_cvt_pk_bf16_f32 v191, v158, v159\n\t"
    "v_exp_f32 v246, v128\n\t"
    "v_exp_f32 v247, v129\n\t"
    "v_mfma_f32_16x16x32_bf16 v[104:107], a[148:151], v[200:203], 0\n\t"
    "v_exp_f32 v248, v130\n\t"
    "v_exp_f32 v249, v131\n\t"
    "v_mfma_f32_16x16x32_bf16 v[108:111], a[148:151], v[204:207], 0\n\t"
    "v_cvt_pk_bf16_f32 v192, v246, v247\n\t"
    "v_exp_f32 v246, v132\n\t"
    "v_mfma_f32_16x16x32_bf16 v[112:115], a[152:155], v[200:203], 0\n\t"
    "v_exp_f32 v247, v133\n\t"
    "v_cvt_pk_bf16_f32 v193, v248, v249\n\t"
    "v_mfma_f32_16x16x32_bf16 v[116:119], a[152:155], v[204:207], 0\n\t"
    "buffer_load_dwordx4 v243, s[28:31], s57 offen offset:3072 nt lds\n\t"
    "v_exp_f32 v248, v134\n\t"
    "v_exp_f32 v249, v135\n\t"
    "v_mfma_f32_16x16x32_bf16 v[120:123], a[156:159], v[200:203], 0\n\t"
    "v_cvt_pk_bf16_f32 v194, v246, v247\n\t"
    "v_exp_f32 v246, v136\n\t"
    "v_exp_f32 v247, v137\n\t"
    "v_mfma_f32_16x16x32_bf16 v[124:127], a[156:159], v[204:207], 0\n\t"
    "v_cvt_pk_bf16_f32 v195, v248, v249\n\t"
    "s_nop 0\n\t"
    "v_cvt_pk_bf16_f32 v196, v246, v247\n\t"
    "v_mfma_f32_16x16x32_bf16 v[216:219], v[224:227], v[184:187], v[216:219]\n\t"
    "v_mfma_f32_16x16x32_bf16 v[220:223], v[224:227], v[188:191], v[220:223]\n\t"
    "s_add_u32 m0, s41, 32768\n\t"
    "s_add_u32 s57, s49, 32768\n\t"
    "buffer_load_dwordx4 v244, s[32:35], s57 offen offset:0 nt lds\n\t"
    "buffer_load_dwordx4 v245, s[32:35], s57 offen offset:1024 nt lds\n\t"
    "buffer_load_dwordx4 v244, s[32:35], s57 offen offset:2048 nt lds\n\t"
    "buffer_load_dwordx4 v245, s[32:35], s57 offen offset:3072 nt lds\n\t"
    "s_add_u32 m0, s40, 0\n\t"
    "s_add_u32 s57, s49, 65536\n\t"
    "buffer_load_dwordx4 v240, s[28:31], s57 offen offset:0 nt lds\n\t"
    "buffer_load_dwordx4 v241, s[28:31], s57 offen offset:1024 nt lds\n\t"
    "buffer_load_dwordx4 v242, s[28:31], s57 offen offset:2048 nt lds\n\t"
    "buffer_load_dwordx4 v243, s[28:31], s57 offen offset:3072 nt lds\n\t"
    "v_mfma_f32_16x16x32_bf16 v[144:147], a[64:67], v[32:35], v[168:171]\n\t"
    "v_exp_f32 v248, v138\n\t"
    "v_mfma_f32_16x16x32_bf16 v[144:147], a[68:71], v[36:39], v[144:147]\n\t"
    "s_waitcnt vmcnt(12) lgkmcnt(0)\n\t"
    "s_barrier\n\t"
    "s_memrealtime s[92:93]\n\t"
    "v_mfma_f32_16x16x32_bf16 v[144:147], a[72:75], v[40:43], v[144:147]\n\t"
    "v_exp_f32 v249, v139\n\t"
    "v_mfma_f32_16x16x32_bf16 v[144:147], a[76:79], v[44:47], v[144:147]\n\t"
    "s_add_u32 m0, s41, 49152\n\t"
    "s_nop 0\n\t"
    "buffer_load_dwordx4 v244, s[32:35], s43 offen offset:0 nt lds\n\t"
    "v_cvt_pk_bf16_f32 v197, v248, v249\n\t"
    "v_add_f32 v252, v252, v216\n\t"
    "s_waitcnt lgkmcnt(0)\n\t"
    "v_mfma_f32_16x16x32_bf16 v[152:155], a[64:67], v[48:51], v[172:175]\n\t"
    "v_exp_f32 v246, v140\n\t"
    "v_mfma_f32_16x16x32_bf16 v[208:211], v[224:227], v[192:195], 0\n\t"
    "ds_read_b128 a[64:67], v228 offset:32768\n\t"
    "v_mfma_f32_16x16x32_bf16 v[152:155], a[68:71], v[52:55], v[152:155]\n\t"
    "s_add_u32 m0, s41, 49152\n\t"
    "s_nop 0\n\t"
    "buffer_load_dwordx4 v245, s[32:35], s43 offen offset:1024 nt lds\n\t"
    "v_mfma_f32_16x16x32_bf16 v[152:155], a[72:75], v[56:59], v[152:155]\n\t"
    "v_exp_f32 v247, v141\n\t"
    "v_mfma_f32_16x16x32_bf16 v[152:155], a[76:79], v[60:63], v[152:155]\n\t"
    "ds_read_b128 a[68:71], v229 offset:32768\n\t"
    "v_cvt_pk_bf16_f32 v198, v246, v247\n\t"
    "v_mfma_f32_16x16x32_bf16 v[148:151], a[80:83], v[32:35], v[168:171]\n\t"
    "v_exp_f32 v248, v142\n\t"
    "v_mfma_f32_16x16x32_bf16 v[148:151], a[84:87], v[36:39], v[148:151]\n\t"
    "ds_read_b128 a[72:75], v230 offset:32768\n\t"
    "ds_read_b128 a[76:79], v231 offset:32768\n\t"
    "s_add_u32 m0, s41, 49152\n\t"
    "s_nop 0\n\t"
    "buffer_load_dwordx4 v244, s[32:35], s43 offen offset:2048 nt lds\n\t"
    "v_mfma_f32_16x16x32_bf16 v[148:151], a[88:91], v[40:43], v[148:151]\n\t"
    "v_exp_f32 v249, v143\n\t"
    "v_mfma_f32_16x16x32_bf16 v[148:151], a[92:95], v[44:47], v[148:151]\n\t"
    "v_cvt_pk_bf16_f32 v199, v248, v249\n\t"
    "v_add_f32 v253, v253, v220\n\t"
    "v_mfma_f32_16x16x32_bf16 v[156:159], a[80:83], v[48:51], v[172:175]\n\t"
    "v_exp_f32 v246, v144\n\t"
    "v_mfma_f32_16x16x32_bf16 v[212:215], v[224:227], v[196:199], 0\n\t"
    "ds_read_b128 a[80:83], v228 offset:36864\n\t"
    "v_mfma_f32_16x16x32_bf16 v[156:159], a[84:87], v[52:55], v[156:159]\n\t"
    "s_add_u32 m0, s41, 49152\n\t"
    "s_nop 0\n\t"
    "buffer_load_dwordx4 v245, s[32:35], s43 offen offset:3072 nt lds\n\t"
    "v_mfma_f32_16x16x32_bf16 v[156:159], a[88:91], v[56:59], v[156:159]\n\t"
    "v_exp_f32 v247, v145\n\t"
    "v_mfma_f32_16x16x32_bf16 v[156:159], a[92:95], v[60:63], v[156:159]\n\t"
    "ds_read_b128 a[84:87], v229 offset:36864\n\t"
    "v_cvt_pk_bf16_f32 v200, v246, v247\n\t"
    "v_mfma_f32_16x16x32_bf16 v[64:67], a[96:99], v[184:187], v[64:67]\n\t"
    "v_exp_f32 v248, v146\n\t"
    "v_mfma_f32_16x16x32_bf16 v[68:71], a[96:99], v[188:191], v[68:71]\n\t"
    "s_add_u32 m0, s40, 16384\n\t"
    "s_add_u32 s42, s42, 16384\n\t"
    "buffer_load_dwordx4 v240, s[28:31], s42 offen offset:0 nt lds\n\t"
    "v_max_f32 v254, v208, v212\n\t"
    "v_cmp_lt_f32 vcc, s45, v254\n\t"
    "v_mfma_f32_16x16x32_bf16 v[72:75], a[100:103], v[184:187], v[72:75]\n\t"
    "v_exp_f32 v249, v147\n\t"
    "v_mfma_f32_16x16x32_bf16 v[76:79], a[100:103], v[188:191], v[76:79]\n\t"
    "ds_read_b128 a[88:91], v230 offset:36864\n\t"
    "v_cvt_pk_bf16_f32 v201, v248, v249\n\t"
    "v_mfma_f32_16x16x32_bf16 v[80:83], a[104:107], v[184:187], v[80:83]\n\t"
    "v_exp_f32 v246, v148\n\t"
    "v_mfma_f32_16x16x32_bf16 v[84:87], a[104:107], v[188:191], v[84:87]\n\t"
    "s_add_u32 m0, s40, 16384\n\t"
    "s_nop 0\n\t"
    "buffer_load_dwordx4 v241, s[28:31], s42 offen offset:1024 nt lds\n\t"
    "v_mfma_f32_16x16x32_bf16 v[88:91], a[108:111], v[184:187], v[88:91]\n\t"
    "v_exp_f32 v247, v149\n\t"
    "v_mfma_f32_16x16x32_bf16 v[92:95], a[108:111], v[188:191], v[92:95]\n\t"
    "ds_read_b128 a[92:95], v231 offset:36864\n\t"
    "v_cvt_pk_bf16_f32 v202, v246, v247\n\t"
    "v_mfma_f32_16x16x32_bf16 v[96:99], a[112:115], v[184:187], v[96:99]\n\t"
    "v_exp_f32 v248, v150\n\t"
    "v_mfma_f32_16x16x32_bf16 v[100:103], a[112:115], v[188:191], v[100:103]\n\t"
    "s_add_u32 m0, s40, 16384\n\t"
    "s_nop 0\n\t"
    "buffer_load_dwordx4 v242, s[28:31], s42 offen offset:2048 nt lds\n\t"
    "s_cbranch_vccnz .Ltsv_stub0\n\t"
    ".Ltsv_back0:\n\t"
    "v_mfma_f32_16x16x32_bf16 v[104:107], a[116:119], v[184:187], v[104:107]\n\t"
    "v_exp_f32 v249, v151\n\t"
    "v_mfma_f32_16x16x32_bf16 v[108:111], a[116:119], v[188:191], v[108:111]\n\t"
    "ds_read_b64_tr_b16 a[96:97], v232 offset:16384\n\t"
    "v_cvt_pk_bf16_f32 v203, v248, v249\n\t"
    "v_mfma_f32_16x16x32_bf16 v[112:115], a[120:123], v[184:187], v[112:115]\n\t"
    "v_exp_f32 v246, v152\n\t"
    "v_mfma_f32_16x16x32_bf16 v[216:219], v[224:227], v[200:203], 0\n\t"
    "ds_read_b64_tr_b16 a[98:99], v232 offset:20480\n\t"
    "ds_read_b64_tr_b16 a[100:101], v233 offset:16384\n\t"
    "v_mfma_f32_16x16x32_bf16 v[116:119], a[120:123], v[188:191], v[116:119]\n\t"
    "s_add_u32 m0, s40, 16384\n\t"
    "s_nop 0\n\t"
    "buffer_load_dwordx4 v243, s[28:31], s42 offen offset:3072 nt lds\n\t"
    "v_mfma_f32_16x16x32_bf16 v[120:123], a[124:127], v[184:187], v[120:123]\n\t"
    "v_exp_f32 v247, v153\n\t"
    "v_mfma_f32_16x16x32_bf16 v[124:127], a[124:127], v[188:191], v[124:127]\n\t"
    "s_waitcnt lgkmcnt(3)\n\t"
    "ds_read_b64_tr_b16 a[102:103], v233 offset:20480\n\t"
    "v_cvt_pk_bf16_f32 v204, v246, v247\n\t"
    "s_branch .Ltsv_loop\n\t"
    ".Ltsv_dly2:\n\t"
    "s_add_u32 s92, s92, s94\n\t"
    "s_add_u32 s92, s92, 66\n\t"
    "s_sub_i32 s94, s94, 4\n\t"
    ".Ltsv_dl2:\n\t"
    "s_memrealtime s[96:97]\n\t"
    "s_waitcnt lgkmcnt(0)\n\t"
    "s_sub_u32 s93, s92, s96\n\t"
    "s_cmp_gt_i32 s93, 0\n\t"
    "s_cbranch_scc1 .Ltsv_dl2\n\t"
    "s_branch .Ltsv_back2\n\t"
    ".Ltsv_dly5:\n\t"
    "s_add_u32 s92, s92, s94\n\t"
    "s_add_u32 s92, s92, 66\n\t"
    "s_sub_i32 s94, s94, 4\n\t"
    ".Ltsv_dl5:\n\t"
    "s_memrealtime s[96:97]\n\t"
    "s_waitcnt lgkmcnt(0)\n\t"
    "s_sub_u32 s93, s92, s96\n\t"
    "s_cmp_gt_i32 s93, 0\n\t"
    "s_cbranch_scc1 .Ltsv_dl5\n\t"
    "s_branch .Ltsv_back5\n\t"
    ".Ltsv_dly8:\n\t"
    "s_add_u32 s92, s92, s94\n\t"
    "s_add_u32 s92, s92, 66\n\t"
    "s_sub_i32 s94, s94, 4\n\t"
    ".Ltsv_dl8:\n\t"
    "s_memrealtime s[96:97]\n\t"
    "s_waitcnt lgkmcnt(0)\n\t"
    "s_sub_u32 s93, s92, s96\n\t"
    "s_cmp_gt_i32 s93, 0\n\t"
    "s_cbranch_scc1 .Ltsv_dl8\n\t"
    "s_branch .Ltsv_back8\n\t"
    ".Ltsv_dly11:\n\t"
    "s_add_u32 s92, s92, s94\n\t"
    "s_add_u32 s92, s92, 66\n\t"
    "s_sub_i32 s94, s94, 4\n\t"
    ".Ltsv_dl11:\n\t"
    "s_memrealtime s[96:97]\n\t"
    "s_waitcnt lgkmcnt(0)\n\t"
    "s_sub_u32 s93, s92, s96\n\t"
    "s_cmp_gt_i32 s93, 0\n\t"
    "s_cbranch_scc1 .Ltsv_dl11\n\t"
    "s_branch .Ltsv_back11\n\t"
    ".Ltsv_loop:\n\t"
    "v_mfma_f32_16x16x32_bf16 v[128:131], a[64:67], v[0:3], v[160:163]\n\t"
    "v_exp_f32 v248, v154\n\t"
    "v_mfma_f32_16x16x32_bf16 v[128:131], a[68:71], v[4:7], v[128:131]\n\t"
    "ds_read_b64_tr_b16 a[104:105], v234 offset:16384\n\t"
    "ds_read_b64_tr_b16 a[106:107], v234 offset:20480\n\t"
    "v_mfma_f32_16x16x32_bf16 v[128:131], a[72:75], v[8:11], v[128:131]\n\t"
    "s_add_u32 s43, s43, 16384\n\t"
    "v_exp_f32 v249, v155\n\t"
    "v_mfma_f32_16x16x32_bf16 v[128:131], a[76:79], v[12:15], v[128:131]\n\t"
    "ds_read_b64_tr_b16 a[108:109], v235 offset:16384\n\t"
    "v_cvt_pk_bf16_f32 v205, v248, v249\n\t"
    "v_mfma_f32_16x16x32_bf16 v[136:139], a[64:67], v[16:19], v[164:167]\n\t"
    "s_add_u32 m0, s41, 0\n\t"
    "v_exp_f32 v246, v156\n\t"
    "v_mfma_f32_16x16x32_bf16 v[136:139], a[68:71], v[20:23], v[136:139]\n\t"
    "buffer_load_dwordx4 v244, s[32:35], s43 offen offset:0 nt lds\n\t"
    "v_mfma_f32_16x16x32_bf16 v[136:139], a[72:75], v[24:27], v[136:139]\n\t"
    "v_exp_f32 v247, v157\n\t"
    "v_mfma_f32_16x16x32_bf16 v[136:139], a[76:79], v[28:31], v[136:139]\n\t"
    "ds_read_b64_tr_b16 a[110:111], v235 offset:20480\n\t"
    "v_cvt_pk_bf16_f32 v206, v246, v247\n\t"
    "v_mfma_f32_16x16x32_bf16 v[132:135], a[80:83], v[0:3], v[160:163]\n\t"
    "v_exp_f32 v248, v158\n\t"
    "v_mfma_f32_16x16x32_bf16 v[132:135], a[84:87], v[4:7], v[132:135]\n\t"
    "ds_read_b64_tr_b16 a[112:113], v236 offset:16384\n\t"
    "ds_read_b64_tr_b16 a[114:115], v236 offset:20480\n\t"
    "v_mfma_f32_16x16x32_bf16 v[132:135], a[88:91], v[8:11], v[132:135]\n\t"
    "v_exp_f32 v249, v159\n\t"
    "v_mfma_f32_16x16x32_bf16 v[132:135], a[92:95], v[12:15], v[132:135]\n\t"
    "ds_read_b64_tr_b16 a[116:117], v237 offset:16384\n\t"
    "v_cvt_pk_bf16_f32 v207, v248, v249\n\t"
    "v_mfma_f32_16x16x32_bf16 v[140:143], a[80:83], v[16:19], v[164:167]\n\t"
    "v_exp_f32 v246, v128\n\t"
    "v_mfma_f32_16x16x32_bf16 v[220:223], v[224:227], v[204:207], 0\n\t"
    "ds_read_b64_tr_b16 a[118:119], v237 offset:20480\n\t"
    "ds_read_b64_tr_b16 a[120:121], v238 offset:16384\n\t"
    "v_mfma_f32_16x16x32_bf16 v[140:143], a[84:87], v[20:23], v[140:143]\n\t"
    "buffer_load_dwordx4 v245, s[32:35], s43 offen offset:1024 nt lds\n\t"
    "v_mfma_f32_16x16x32_bf16 v[140:143], a[88:91], v[24:27], v[140:143]\n\t"
    "v_exp_f32 v247, v129\n\t"
    "v_mfma_f32_16x16x32_bf16 v[140:143], a[92:95], v[28:31], v[140:143]\n\t"
    "s_waitcnt lgkmcnt(5)\n\t"
    "ds_read_b64_tr_b16 a[122:123], v238 offset:20480\n\t"
    "v_cvt_pk_bf16_f32 v176, v246, v247\n\t"
    "v_mfma_f32_16x16x32_bf16 a[0:3], a[96:99], v[192:195], a[0:3]\n\t"
    "v_exp_f32 v248, v130\n\t"
    "v_mfma_f32_16x16x32_bf16 a[4:7], a[96:99], v[196:199], a[4:7]\n\t"
    "v_max_f32 v254, v216, v220\n\t"
    "v_cmp_lt_f32 vcc, s45, v254\n\t"
    "v_mfma_f32_16x16x32_bf16 a[8:11], a[100:103], v[192:195], a[8:11]\n\t"
    "v_exp_f32 v249, v131\n\t"
    "v_mfma_f32_16x16x32_bf16 a[12:15], a[100:103], v[196:199], a[12:15]\n\t"
    "ds_read_b64_tr_b16 a[124:125], v239 offset:16384\n\t"
    "v_cvt_pk_bf16_f32 v177, v248, v249\n\t"
    "v_mfma_f32_16x16x32_bf16 a[16:19], a[104:107], v[192:195], a[16:19]\n\t"
    "v_exp_f32 v246, v132\n\t"
    "v_mfma_f32_16x16x32_bf16 a[20:23], a[104:107], v[196:199], a[20:23]\n\t"
    "buffer_load_dwordx4 v244, s[32:35], s43 offen offset:2048 nt lds\n\t"
    "v_mfma_f32_16x16x32_bf16 a[24:27], a[108:111], v[192:195], a[24:27]\n\t"
    "v_exp_f32 v247, v133\n\t"
    "v_mfma_f32_16x16x32_bf16 a[28:31], a[108:111], v[196:199], a[28:31]\n\t"
    "s_waitcnt lgkmcnt(1)\n\t"
    "ds_read_b64_tr_b16 a[126:127], v239 offset:20480\n\t"
    "v_cvt_pk_bf16_f32 v178, v246, v247\n\t"
    "v_mfma_f32_16x16x32_bf16 a[32:35], a[112:115], v[192:195], a[32:35]\n\t"
    "v_exp_f32 v248, v134\n\t"
    "v_mfma_f32_16x16x32_bf16 a[36:39], a[112:115], v[196:199], a[36:39]\n\t"
    "s_cbranch_vccnz .Ltsv_stub1\n\t"
    ".Ltsv_back1:\n\t"
    "v_mfma_f32_16x16x32_bf16 a[40:43], a[116:119], v[192:195], a[40:43]\n\t"
    "v_exp_f32 v249, v135\n\t"
    "v_mfma_f32_16x16x32_bf16 a[44:47], a[116:119], v[196:199], a[44:47]\n\t"
    "v_cvt_pk_bf16_f32 v179, v248, v249\n\t"
    "v_add_f32 v250, v250, v208\n\t"
    "v_mfma_f32_16x16x32_bf16 a[48:51], a[120:123], v[192:195], a[48:51]\n\t"
    "v_exp_f32 v246, v136\n\t"
    "v_mfma_f32_16x16x32_bf16 a[52:55], a[120:123], v[196:199], a[52:55]\n\t"
    "s_waitcnt lgkmcnt(0)\n\t"
    "buffer_load_dwordx4 v245, s[32:35], s43 offen offset:3072 nt lds\n\t"
    "v_mfma_f32_16x16x32_bf16 a[56:59], a[124:127], v[192:195], a[56:59]\n\t"
    "v_exp_f32 v247, v137\n\t"
    "v_mfma_f32_16x16x32_bf16 a[60:63], a[124:127], v[196:199], a[60:63]\n\t"
    "v_cvt_pk_bf16_f32 v180, v246, v247\n\t"
    "v_add_f32 v251, v251, v212\n\t"
    "v_mfma_f32_16x16x32_bf16 v[144:147], a[64:67], v[32:35], v[168:171]\n\t"
    "s_cmp_gt_i32 s94, 0\n\t"
    "v_exp_f32 v248, v138\n\t"
    "v_mfma_f32_16x16x32_bf16 v[144:147], a[68:71], v[36:39], v[144:147]\n\t"
    "s_cbranch_scc1 .Ltsv_dly2\n\t"
    ".Ltsv_back2:\n\t"
    "s_waitcnt vmcnt(16)\n\t"
    "s_barrier\n\t"
    "v_mfma_f32_16x16x32_bf16 v[144:147], a[72:75], v[40:43], v[144:147]\n\t"
    "s_add_u32 s42, s42, 16384\n\t"
    "v_exp_f32 v249, v139\n\t"
    "v_mfma_f32_16x16x32_bf16 v[144:147], a[76:79], v[44:47], v[144:147]\n\t"
    "v_cvt_pk_bf16_f32 v181, v248, v249\n\t"
    "v_add_f32 v252, v252, v216\n\t"
    "v_mfma_f32_16x16x32_bf16 v[152:155], a[64:67], v[48:51], v[172:175]\n\t"
    "s_add_u32 m0, s40, 32768\n\t"
    "v_exp_f32 v246, v140\n\t"
    "v_mfma_f32_16x16x32_bf16 v[208:211], v[224:227], v[176:179], 0\n\t"
    "ds_read_b128 a[64:67], v228 offset:49152\n\t"
    "v_mfma_f32_16x16x32_bf16 v[152:155], a[68:71], v[52:55], v[152:155]\n\t"
    "buffer_load_dwordx4 v240, s[28:31], s42 offen offset:0 nt lds\n\t"
    "v_mfma_f32_16x16x32_bf16 v[152:155], a[72:75], v[56:59], v[152:155]\n\t"
    "v_exp_f32 v247, v141\n\t"
    "v_mfma_f32_16x16x32_bf16 v[152:155], a[76:79], v[60:63], v[152:155]\n\t"
    "ds_read_b128 a[68:71], v229 offset:49152\n\t"
    "v_cvt_pk_bf16_f32 v182, v246, v247\n\t"
    "v_mfma_f32_16x16x32_bf16 v[148:151], a[80:83], v[32:35], v[168:171]\n\t"
    "v_exp_f32 v248, v142\n\t"
    "v_mfma_f32_16x16x32_bf16 v[148:151], a[84:87], v[36:39], v[148:151]\n\t"
    "ds_read_b128 a[72:75], v230 offset:49152\n\t"
    "ds_read_b128 a[76:79], v231 offset:49152\n\t"
    "v_mfma_f32_16x16x32_bf16 v[148:151], a[88:91], v[40:43], v[148:151]\n\t"
    "v_exp_f32 v249, v143\n\t"
    "v_mfma_f32_16x16x32_bf16 v[148:151], a[92:95], v[44:47], v[148:151]\n\t"
    "v_cvt_pk_bf16_f32 v183, v248, v249\n\t"
    "v_add_f32 v253, v253, v220\n\t"
    "v_mfma_f32_16x16x32_bf16 v[156:159], a[80:83], v[48:51], v[172:175]\n\t"
    "v_exp_f32 v246, v144\n\t"
    "v_mfma_f32_16x16x32_bf16 v[212:215], v[224:227], v[180:183], 0\n\t"
    "ds_read_b128 a[80:83], v228 offset:53248\n\t"
    "v_mfma_f32_16x16x32_bf16 v[156:159], a[84:87], v[52:55], v[156:159]\n\t"
    "buffer_load_dwordx4 v241, s[28:31], s42 offen offset:1024 nt lds\n\t"
    "v_mfma_f32_16x16x32_bf16 v[156:159], a[88:91], v[56:59], v[156:159]\n\t"
    "v_exp_f32 v247, v145\n\t"
    "v_mfma_f32_16x16x32_bf16 v[156:159], a[92:95], v[60:63], v[156:159]\n\t"
    "ds_read_b128 a[84:87], v229 offset:53248\n\t"
    "v_cvt_pk_bf16_f32 v184, v246, v247\n\t"
    "v_mfma_f32_16x16x32_bf16 v[64:67], a[96:99], v[200:203], v[64:67]\n\t"
    "v_exp_f32 v248, v146\n\t"
    "v_mfma_f32_16x16x32_bf16 v[68:71], a[96:99], v[204:207], v[68:71]\n\t"
    "v_max_f32 v254, v208, v212\n\t"
    "v_cmp_lt_f32 vcc, s45, v254\n\t"
    "v_mfma_f32_16x16x32_bf16 v[72:75], a[100:103], v[200:203], v[72:75]\n\t"
    "v_exp_f32 v249, v147\n\t"
    "v_mfma_f32_16x16x32_bf16 v[76:79], a[100:103], v[204:207], v[76:79]\n\t"
    "ds_read_b128 a[88:91], v230 offset:53248\n\t"
    "v_cvt_pk_bf16_f32 v185, v248, v249\n\t"
    "v_mfma_f32_16x16x32_bf16 v[80:83], a[104:107], v[200:203], v[80:83]\n\t"
    "v_exp_f32 v246, v148\n\t"
    "v_mfma_f32_16x16x32_bf16 v[84:87], a[104:107], v[204:207], v[84:87]\n\t"
    "buffer_load_dwordx4 v242, s[28:31], s42 offen offset:2048 nt lds\n\t"
    "v_mfma_f32_16x16x32_bf16 v[88:91], a[108:111], v[200:203], v[88:91]\n\t"
    "v_exp_f32 v247, v149\n\t"
    "v_mfma_f32_16x16x32_bf16 v[92:95], a[108:111], v[204:207], v[92:95]\n\t"
    "ds_read_b128 a[92:95], v231 offset:53248\n\t"
    "v_cvt_pk_bf16_f32 v186, v246, v247\n\t"
    "v_mfma_f32_16x16x32_bf16 v[96:99], a[112:115], v[200:203], v[96:99]\n\t"
    "v_exp_f32 v248, v150\n\t"
    "v_mfma_f32_16x16x32_bf16 v[100:103], a[112:115], v[204:207], v[100:103]\n\t"
    "s_cbranch_vccnz .Ltsv_stub3\n\t"
    ".Ltsv_back3:\n\t"
    "v_mfma_f32_16x16x32_bf16 v[104:107], a[116:119], v[200:203], v[104:107]\n\t"
    "v_exp_f32 v249, v151\n\t"
    "v_mfma_f32_16x16x32_bf16 v[108:111], a[116:119], v[204:207], v[108:111]\n\t"
    "ds_read_b64_tr_b16 a[96:97], v232 offset:32768\n\t"
    "v_cvt_pk_bf16_f32 v187, v248, v249\n\t"
    "v_mfma_f32_16x16x32_bf16 v[112:115], a[120:123], v[200:203], v[112:115]\n\t"
    "v_exp_f32 v246, v152\n\t"
    "v_mfma_f32_16x16x32_bf16 v[216:219], v[224:227], v[184:187], 0\n\t"
    "ds_read_b64_tr_b16 a[98:99], v232 offset:36864\n\t"
    "ds_read_b64_tr_b16 a[100:101], v233 offset:32768\n\t"
    "v_mfma_f32_16x16x32_bf16 v[116:119], a[120:123], v[204:207], v[116:119]\n\t"
    "buffer_load_dwordx4 v243, s[28:31], s42 offen offset:3072 nt lds\n\t"
    "v_mfma_f32_16x16x32_bf16 v[120:123], a[124:127], v[200:203], v[120:123]\n\t"
    "v_exp_f32 v247, v153\n\t"
    "v_mfma_f32_16x16x32_bf16 v[124:127], a[124:127], v[204:207], v[124:127]\n\t"
    "s_waitcnt lgkmcnt(3)\n\t"
    "ds_read_b64_tr_b16 a[102:103], v233 offset:36864\n\t"
    "v_cvt_pk_bf16_f32 v188, v246, v247\n\t"
    "v_mfma_f32_16x16x32_bf16 v[128:131], a[64:67], v[0:3], v[160:163]\n\t"
    "v_exp_f32 v248, v154\n\t"
    "v_mfma_f32_16x16x32_bf16 v[128:131], a[68:71], v[4:7], v[128:131]\n\t"
    "ds_read_b64_tr_b16 a[104:105], v234 offset:32768\n\t"
    "ds_read_b64_tr_b16 a[106:107], v234 offset:36864\n\t"
    "v_mfma_f32_16x16x32_bf16 v[128:131], a[72:75], v[8:11], v[128:131]\n\t"
    "s_add_u32 s43, s43, 16384\n\t"
    "v_exp_f32 v249, v155\n\t"
    "v_mfma_f32_16x16x32_bf16 v[128:131], a[76:79], v[12:15], v[128:131]\n\t"
    "ds_read_b64_tr_b16 a[108:109], v235 offset:32768\n\t"
    "v_cvt_pk_bf16_f32 v189, v248, v249\n\t"
    "v_mfma_f32_16x16x32_bf16 v[136:139], a[64:67], v[16:19], v[164:167]\n\t"
    "s_add_u32 m0, s41, 16384\n\t"
    "v_exp_f32 v246, v156\n\t"
    "v_mfma_f32_16x16x32_bf16 v[136:139], a[68:71], v[20:23], v[136:139]\n\t"
    "buffer_load_dwordx4 v244, s[32:35], s43 offen offset:0 nt lds\n\t"
    "v_mfma_f32_16x16x32_bf16 v[136:139], a[72:75], v[24:27], v[136:139]\n\t"
    "v_exp_f32 v247, v157\n\t"
    "v_mfma_f32_16x16x32_bf16 v[136:139], a[76:79], v[28:31], v[136:139]\n\t"
    "ds_read_b64_tr_b16 a[110:111], v235 offset:36864\n\t"
    "v_cvt_pk_bf16_f32 v190, v246, v247\n\t"
    "v_mfma_f32_16x16x32_bf16 v[132:135], a[80:83], v[0:3], v[160:163]\n\t"
    "v_exp_f32 v248, v158\n\t"
    "v_mfma_f32_16x16x32_bf16 v[132:135], a[84:87], v[4:7], v[132:135]\n\t"
    "ds_read_b64_tr_b16 a[112:113], v236 offset:32768\n\t"
    "ds_read_b64_tr_b16 a[114:115], v236 offset:36864\n\t"
    "v_mfma_f32_16x16x32_bf16 v[132:135], a[88:91], v[8:11], v[132:135]\n\t"
    "v_exp_f32 v249, v159\n\t"
    "v_mfma_f32_16x16x32_bf16 v[132:135], a[92:95], v[12:15], v[132:135]\n\t"
    "ds_read_b64_tr_b16 a[116:117], v237 offset:32768\n\t"
    "v_cvt_pk_bf16_f32 v191, v248, v249\n\t"
    "v_mfma_f32_16x16x32_bf16 v[140:143], a[80:83], v[16:19], v[164:167]\n\t"
    "v_exp_f32 v246, v128\n\t"
    "v_mfma_f32_16x16x32_bf16 v[220:223], v[224:227], v[188:191], 0\n\t"
    "ds_read_b64_tr_b16 a[118:119], v237 offset:36864\n\t"
    "ds_read_b64_tr_b16 a[120:121], v238 offset:32768\n\t"
    "v_mfma_f32_16x16x32_bf16 v[140:143], a[84:87], v[20:23], v[140:143]\n\t"
    "buffer_load_dwordx4 v245, s[32:35], s43 offen offset:1024 nt lds\n\t"
    "v_mfma_f32_16x16x32_bf16 v[140:143], a[88:91], v[24:27], v[140:143]\n\t"
    "v_exp_f32 v247, v129\n\t"
    "v_mfma_f32_16x16x32_bf16 v[140:143], a[92:95], v[28:31], v[140:143]\n\t"
    "s_waitcnt lgkmcnt(5)\n\t"
    "ds_read_b64_tr_b16 a[122:123], v238 offset:36864\n\t"
    "v_cvt_pk_bf16_f32 v192, v246, v247\n\t"
    "v_mfma_f32_16x16x32_bf16 a[0:3], a[96:99], v[176:179], a[0:3]\n\t"
    "v_exp_f32 v248, v130\n\t"
    "v_mfma_f32_16x16x32_bf16 a[4:7], a[96:99], v[180:183], a[4:7]\n\t"
    "v_max_f32 v254, v216, v220\n\t"
    "v_cmp_lt_f32 vcc, s45, v254\n\t"
    "v_mfma_f32_16x16x32_bf16 a[8:11], a[100:103], v[176:179], a[8:11]\n\t"
    "v_exp_f32 v249, v131\n\t"
    "v_mfma_f32_16x16x32_bf16 a[12:15], a[100:103], v[180:183], a[12:15]\n\t"
    "ds_read_b64_tr_b16 a[124:125], v239 offset:32768\n\t"
    "v_cvt_pk_bf16_f32 v193, v248, v249\n\t"
    "v_mfma_f32_16x16x32_bf16 a[16:19], a[104:107], v[176:179], a[16:19]\n\t"
    "v_exp_f32 v246, v132\n\t"
    "v_mfma_f32_16x16x32_bf16 a[20:23], a[104:107], v[180:183], a[20:23]\n\t"
    "buffer_load_dwordx4 v244, s[32:35], s43 offen offset:2048 nt lds\n\t"
    "v_mfma_f32_16x16x32_bf16 a[24:27], a[108:111], v[176:179], a[24:27]\n\t"
    "v_exp_f32 v247, v133\n\t"
    "v_mfma_f32_16x16x32_bf16 a[28:31], a[108:111], v[180:183], a[28:31]\n\t"
    "s_waitcnt lgkmcnt(1)\n\t"
    "ds_read_b64_tr_b16 a[126:127], v239 offset:36864\n\t"
    "v_cvt_pk_bf16_f32 v194, v246, v247\n\t"
    "v_mfma_f32_16x16x32_bf16 a[32:35], a[112:115], v[176:179], a[32:35]\n\t"
    "v_exp_f32 v248, v134\n\t"
    "v_mfma_f32_16x16x32_bf16 a[36:39], a[112:115], v[180:183], a[36:39]\n\t"
    "s_cbranch_vccnz .Ltsv_stub4\n\t"
    ".Ltsv_back4:\n\t"
    "v_mfma_f32_16x16x32_bf16 a[40:43], a[116:119], v[176:179], a[40:43]\n\t"
    "v_exp_f32 v249, v135\n\t"
    "v_mfma_f32_16x16x32_bf16 a[44:47], a[116:119], v[180:183], a[44:47]\n\t"
    "v_cvt_pk_bf16_f32 v195, v248, v249\n\t"
    "v_add_f32 v250, v250, v208\n\t"
    "v_mfma_f32_16x16x32_bf16 a[48:51], a[120:123], v[176:179], a[48:51]\n\t"
    "v_exp_f32 v246, v136\n\t"
    "v_mfma_f32_16x16x32_bf16 a[52:55], a[120:123], v[180:183], a[52:55]\n\t"
    "s_waitcnt lgkmcnt(0)\n\t"
    "buffer_load_dwordx4 v245, s[32:35], s43 offen offset:3072 nt lds\n\t"
    "v_mfma_f32_16x16x32_bf16 a[56:59], a[124:127], v[176:179], a[56:59]\n\t"
    "v_exp_f32 v247, v137\n\t"
    "v_mfma_f32_16x16x32_bf16 a[60:63], a[124:127], v[180:183], a[60:63]\n\t"
    "v_cvt_pk_bf16_f32 v196, v246, v247\n\t"
    "v_add_f32 v251, v251, v212\n\t"
    "v_mfma_f32_16x16x32_bf16 v[144:147], a[64:67], v[32:35], v[168:171]\n\t"
    "s_cmp_gt_i32 s94, 0\n\t"
    "v_exp_f32 v248, v138\n\t"
    "v_mfma_f32_16x16x32_bf16 v[144:147], a[68:71], v[36:39], v[144:147]\n\t"
    "s_cbranch_scc1 .Ltsv_dly5\n\t"
    ".Ltsv_back5:\n\t"
    "s_waitcnt vmcnt(16)\n\t"
    "s_barrier\n\t"
    "v_mfma_f32_16x16x32_bf16 v[144:147], a[72:75], v[40:43], v[144:147]\n\t"
    "s_add_u32 s42, s42, 16384\n\t"
    "v_exp_f32 v249, v139\n\t"
    "v_mfma_f32_16x16x32_bf16 v[144:147], a[76:79], v[44:47], v[144:147]\n\t"
    "v_cvt_pk_bf16_f32 v197, v248, v249\n\t"
    "v_add_f32 v252, v252, v216\n\t"
    "v_mfma_f32_16x16x32_bf16 v[152:155], a[64:67], v[48:51], v[172:175]\n\t"
    "s_add_u32 m0, s40, 49152\n\t"
    "v_exp_f32 v246, v140\n\t"
    "v_mfma_f32_16x16x32_bf16 v[208:211], v[224:227], v[192:195], 0\n\t"
    "ds_read_b128 a[64:67], v228 offset:0\n\t"
    "v_mfma_f32_16x16x32_bf16 v[152:155], a[68:71], v[52:55], v[152:155]\n\t"
    "buffer_load_dwordx4 v240, s[28:31], s42 offen offset:0 nt lds\n\t"
    "v_mfma_f32_16x16x32_bf16 v[152:155], a[72:75], v[56:59], v[152:155]\n\t"
    "v_exp_f32 v247, v141\n\t"
    "v_mfma_f32_16x16x32_bf16 v[152:155], a[76:79], v[60:63], v[152:155]\n\t"
    "ds_read_b128 a[68:71], v229 offset:0\n\t"
    "v_cvt_pk_bf16_f32 v198, v246, v247\n\t"
    "v_mfma_f32_16x16x32_bf16 v[148:151], a[80:83], v[32:35], v[168:171]\n\t"
    "v_exp_f32 v248, v142\n\t"
    "v_mfma_f32_16x16x32_bf16 v[148:151], a[84:87], v[36:39], v[148:151]\n\t"
    "ds_read_b128 a[72:75], v230 offset:0\n\t"
    "ds_read_b128 a[76:79], v231 offset:0\n\t"
    "v_mfma_f32_16x16x32_bf16 v[148:151], a[88:91], v[40:43], v[148:151]\n\t"
    "v_exp_f32 v249, v143\n\t"
    "v_mfma_f32_16x16x32_bf16 v[148:151], a[92:95], v[44:47], v[148:151]\n\t"
    "v_cvt_pk_bf16_f32 v199, v248, v249\n\t"
    "v_add_f32 v253, v253, v220\n\t"
    "v_mfma_f32_16x16x32_bf16 v[156:159], a[80:83], v[48:51], v[172:175]\n\t"
    "v_exp_f32 v246, v144\n\t"
    "v_mfma_f32_16x16x32_bf16 v[212:215], v[224:227], v[196:199], 0\n\t"
    "ds_read_b128 a[80:83], v228 offset:4096\n\t"
    "v_mfma_f32_16x16x32_bf16 v[156:159], a[84:87], v[52:55], v[156:159]\n\t"
    "buffer_load_dwordx4 v241, s[28:31], s42 offen offset:1024 nt lds\n\t"
    "v_mfma_f32_16x16x32_bf16 v[156:159], a[88:91], v[56:59], v[156:159]\n\t"
    "v_exp_f32 v247, v145\n\t"
    "v_mfma_f32_16x16x32_bf16 v[156:159], a[92:95], v[60:63], v[156:159]\n\t"
    "ds_read_b128 a[84:87], v229 offset:4096\n\t"
    "v_cvt_pk_bf16_f32 v200, v246, v247\n\t"
    "v_mfma_f32_16x16x32_bf16 v[64:67], a[96:99], v[184:187], v[64:67]\n\t"
    "v_exp_f32 v248, v146\n\t"
    "v_mfma_f32_16x16x32_bf16 v[68:71], a[96:99], v[188:191], v[68:71]\n\t"
    "v_max_f32 v254, v208, v212\n\t"
    "v_cmp_lt_f32 vcc, s45, v254\n\t"
    "v_mfma_f32_16x16x32_bf16 v[72:75], a[100:103], v[184:187], v[72:75]\n\t"
    "v_exp_f32 v249, v147\n\t"
    "v_mfma_f32_16x16x32_bf16 v[76:79], a[100:103], v[188:191], v[76:79]\n\t"
    "ds_read_b128 a[88:91], v230 offset:4096\n\t"
    "v_cvt_pk_bf16_f32 v201, v248, v249\n\t"
    "v_mfma_f32_16x16x32_bf16 v[80:83], a[104:107], v[184:187], v[80:83]\n\t"
    "v_exp_f32 v246, v148\n\t"
    "v_mfma_f32_16x16x32_bf16 v[84:87], a[104:107], v[188:191], v[84:87]\n\t"
    "buffer_load_dwordx4 v242, s[28:31], s42 offen offset:2048 nt lds\n\t"
    "v_mfma_f32_16x16x32_bf16 v[88:91], a[108:111], v[184:187], v[88:91]\n\t"
    "v_exp_f32 v247, v149\n\t"
    "v_mfma_f32_16x16x32_bf16 v[92:95], a[108:111], v[188:191], v[92:95]\n\t"
    "ds_read_b128 a[92:95], v231 offset:4096\n\t"
    "v_cvt_pk_bf16_f32 v202, v246, v247\n\t"
    "v_mfma_f32_16x16x32_bf16 v[96:99], a[112:115], v[184:187], v[96:99]\n\t"
    "v_exp_f32 v248, v150\n\t"
    "v_mfma_f32_16x16x32_bf16 v[100:103], a[112:115], v[188:191], v[100:103]\n\t"
    "s_cbranch_vccnz .Ltsv_stub6\n\t"
    ".Ltsv_back6:\n\t"
    "v_mfma_f32_16x16x32_bf16 v[104:107], a[116:119], v[184:187], v[104:107]\n\t"
    "v_exp_f32 v249, v151\n\t"
    "v_mfma_f32_16x16x32_bf16 v[108:111], a[116:119], v[188:191], v[108:111]\n\t"
    "ds_read_b64_tr_b16 a[96:97], v232 offset:49152\n\t"
    "v_cvt_pk_bf16_f32 v203, v248, v249\n\t"
    "v_mfma_f32_16x16x32_bf16 v[112:115], a[120:123], v[184:187], v[112:115]\n\t"
    "v_exp_f32 v246, v152\n\t"
    "v_mfma_f32_16x16x32_bf16 v[216:219], v[224:227], v[200:203], 0\n\t"
    "ds_read_b64_tr_b16 a[98:99], v232 offset:53248\n\t"
    "ds_read_b64_tr_b16 a[100:101], v233 offset:49152\n\t"
    "v_mfma_f32_16x16x32_bf16 v[116:119], a[120:123], v[188:191], v[116:119]\n\t"
    "buffer_load_dwordx4 v243, s[28:31], s42 offen offset:3072 nt lds\n\t"
    "v_mfma_f32_16x16x32_bf16 v[120:123], a[124:127], v[184:187], v[120:123]\n\t"
    "v_exp_f32 v247, v153\n\t"
    "v_mfma_f32_16x16x32_bf16 v[124:127], a[124:127], v[188:191], v[124:127]\n\t"
    "s_waitcnt lgkmcnt(3)\n\t"
    "ds_read_b64_tr_b16 a[102:103], v233 offset:53248\n\t"
    "v_cvt_pk_bf16_f32 v204, v246, v247\n\t"
    "v_mfma_f32_16x16x32_bf16 v[128:131], a[64:67], v[0:3], v[160:163]\n\t"
    "v_exp_f32 v248, v154\n\t"
    "v_mfma_f32_16x16x32_bf16 v[128:131], a[68:71], v[4:7], v[128:131]\n\t"
    "ds_read_b64_tr_b16 a[104:105], v234 offset:49152\n\t"
    "ds_read_b64_tr_b16 a[106:107], v234 offset:53248\n\t"
    "v_mfma_f32_16x16x32_bf16 v[128:131], a[72:75], v[8:11], v[128:131]\n\t"
    "s_add_u32 s43, s43, 16384\n\t"
    "v_exp_f32 v249, v155\n\t"
    "v_mfma_f32_16x16x32_bf16 v[128:131], a[76:79], v[12:15], v[128:131]\n\t"
    "ds_read_b64_tr_b16 a[108:109], v235 offset:49152\n\t"
    "v_cvt_pk_bf16_f32 v205, v248, v249\n\t"
    "v_mfma_f32_16x16x32_bf16 v[136:139], a[64:67], v[16:19], v[164:167]\n\t"
    "s_add_u32 m0, s41, 32768\n\t"
    "v_exp_f32 v246, v156\n\t"
    "v_mfma_f32_16x16x32_bf16 v[136:139], a[68:71], v[20:23], v[136:139]\n\t"
    "buffer_load_dwordx4 v244, s[32:35], s43 offen offset:0 nt lds\n\t"
    "v_mfma_f32_16x16x32_bf16 v[136:139], a[72:75], v[24:27], v[136:139]\n\t"
    "v_exp_f32 v247, v157\n\t"
    "v_mfma_f32_16x16x32_bf16 v[136:139], a[76:79], v[28:31], v[136:139]\n\t"
    "ds_read_b64_tr_b16 a[110:111], v235 offset:53248\n\t"
    "v_cvt_pk_bf16_f32 v206, v246, v247\n\t"
    "v_mfma_f32_16x16x32_bf16 v[132:135], a[80:83], v[0:3], v[160:163]\n\t"
    "v_exp_f32 v248, v158\n\t"
    "v_mfma_f32_16x16x32_bf16 v[132:135], a[84:87], v[4:7], v[132:135]\n\t"
    "ds_read_b64_tr_b16 a[112:113], v236 offset:49152\n\t"
    "ds_read_b64_tr_b16 a[114:115], v236 offset:53248\n\t"
    "v_mfma_f32_16x16x32_bf16 v[132:135], a[88:91], v[8:11], v[132:135]\n\t"
    "v_exp_f32 v249, v159\n\t"
    "v_mfma_f32_16x16x32_bf16 v[132:135], a[92:95], v[12:15], v[132:135]\n\t"
    "ds_read_b64_tr_b16 a[116:117], v237 offset:49152\n\t"
    "v_cvt_pk_bf16_f32 v207, v248, v249\n\t"
    "v_mfma_f32_16x16x32_bf16 v[140:143], a[80:83], v[16:19], v[164:167]\n\t"
    "v_exp_f32 v246, v128\n\t"
    "v_mfma_f32_16x16x32_bf16 v[220:223], v[224:227], v[204:207], 0\n\t"
    "ds_read_b64_tr_b16 a[118:119], v237 offset:53248\n\t"
    "ds_read_b64_tr_b16 a[120:121], v238 offset:49152\n\t"
    "v_mfma_f32_16x16x32_bf16 v[140:143], a[84:87], v[20:23], v[140:143]\n\t"
    "buffer_load_dwordx4 v245, s[32:35], s43 offen offset:1024 nt lds\n\t"
    "v_mfma_f32_16x16x32_bf16 v[140:143], a[88:91], v[24:27], v[140:143]\n\t"
    "v_exp_f32 v247, v129\n\t"
    "v_mfma_f32_16x16x32_bf16 v[140:143], a[92:95], v[28:31], v[140:143]\n\t"
    "s_waitcnt lgkmcnt(5)\n\t"
    "ds_read_b64_tr_b16 a[122:123], v238 offset:53248\n\t"
    "v_cvt_pk_bf16_f32 v176, v246, v247\n\t"
    "v_mfma_f32_16x16x32_bf16 a[0:3], a[96:99], v[192:195], a[0:3]\n\t"
    "v_exp_f32 v248, v130\n\t"
    "v_mfma_f32_16x16x32_bf16 a[4:7], a[96:99], v[196:199], a[4:7]\n\t"
    "v_max_f32 v254, v216, v220\n\t"
    "v_cmp_lt_f32 vcc, s45, v254\n\t"
    "v_mfma_f32_16x16x32_bf16 a[8:11], a[100:103], v[192:195], a[8:11]\n\t"
    "v_exp_f32 v249, v131\n\t"
    "v_mfma_f32_16x16x32_bf16 a[12:15], a[100:103], v[196:199], a[12:15]\n\t"
    "ds_read_b64_tr_b16 a[124:125], v239 offset:49152\n\t"
    "v_cvt_pk_bf16_f32 v177, v248, v249\n\t"
    "v_mfma_f32_16x16x32_bf16 a[16:19], a[104:107], v[192:195], a[16:19]\n\t"
    "v_exp_f32 v246, v132\n\t"
    "v_mfma_f32_16x16x32_bf16 a[20:23], a[104:107], v[196:199], a[20:23]\n\t"
    "buffer_load_dwordx4 v244, s[32:35], s43 offen offset:2048 nt lds\n\t"
    "v_mfma_f32_16x16x32_bf16 a[24:27], a[108:111], v[192:195], a[24:27]\n\t"
    "v_exp_f32 v247, v133\n\t"
    "v_mfma_f32_16x16x32_bf16 a[28:31], a[108:111], v[196:199], a[28:31]\n\t"
    "s_waitcnt lgkmcnt(1)\n\t"
    "ds_read_b64_tr_b16 a[126:127], v239 offset:53248\n\t"
    "v_cvt_pk_bf16_f32 v178, v246, v247\n\t"
    "v_mfma_f32_16x16x32_bf16 a[32:35], a[112:115], v[192:195], a[32:35]\n\t"
    "v_exp_f32 v248, v134\n\t"
    "v_mfma_f32_16x16x32_bf16 a[36:39], a[112:115], v[196:199], a[36:39]\n\t"
    "s_cbranch_vccnz .Ltsv_stub7\n\t"
    ".Ltsv_back7:\n\t"
    "v_mfma_f32_16x16x32_bf16 a[40:43], a[116:119], v[192:195], a[40:43]\n\t"
    "v_exp_f32 v249, v135\n\t"
    "v_mfma_f32_16x16x32_bf16 a[44:47], a[116:119], v[196:199], a[44:47]\n\t"
    "v_cvt_pk_bf16_f32 v179, v248, v249\n\t"
    "v_add_f32 v250, v250, v208\n\t"
    "v_mfma_f32_16x16x32_bf16 a[48:51], a[120:123], v[192:195], a[48:51]\n\t"
    "v_exp_f32 v246, v136\n\t"
    "v_mfma_f32_16x16x32_bf16 a[52:55], a[120:123], v[196:199], a[52:55]\n\t"
    "s_waitcnt lgkmcnt(0)\n\t"
    "buffer_load_dwordx4 v245, s[32:35], s43 offen offset:3072 nt lds\n\t"
    "v_mfma_f32_16x16x32_bf16 a[56:59], a[124:127], v[192:195], a[56:59]\n\t"
    "v_exp_f32 v247, v137\n\t"
    "v_mfma_f32_16x16x32_bf16 a[60:63], a[124:127], v[196:199], a[60:63]\n\t"
    "v_cvt_pk_bf16_f32 v180, v246, v247\n\t"
    "v_add_f32 v251, v251, v212\n\t"
    "v_mfma_f32_16x16x32_bf16 v[144:147], a[64:67], v[32:35], v[168:171]\n\t"
    "s_cmp_gt_i32 s94, 0\n\t"
    "v_exp_f32 v248, v138\n\t"
    "v_mfma_f32_16x16x32_bf16 v[144:147], a[68:71], v[36:39], v[144:147]\n\t"
    "s_cbranch_scc1 .Ltsv_dly8\n\t"
    ".Ltsv_back8:\n\t"
    "s_waitcnt vmcnt(16)\n\t"
    "s_barrier\n\t"
    "v_mfma_f32_16x16x32_bf16 v[144:147], a[72:75], v[40:43], v[144:147]\n\t"
    "s_add_u32 s42, s42, 16384\n\t"
    "v_exp_f32 v249, v139\n\t"
    "v_mfma_f32_16x16x32_bf16 v[144:147], a[76:79], v[44:47], v[144:147]\n\t"
    "v_cvt_pk_bf16_f32 v181, v248, v249\n\t"
    "v_add_f32 v252, v252, v216\n\t"
    "v_mfma_f32_16x16x32_bf16 v[152:155], a[64:67], v[48:51], v[172:175]\n\t"
    "s_add_u32 m0, s40, 0\n\t"
    "v_exp_f32 v246, v140\n\t"
    "v_mfma_f32_16x16x32_bf16 v[208:211], v[224:227], v[176:179], 0\n\t"
    "ds_read_b128 a[64:67], v228 offset:16384\n\t"
    "v_mfma_f32_16x16x32_bf16 v[152:155], a[68:71], v[52:55], v[152:155]\n\t"
    "buffer_load_dwordx4 v240, s[28:31], s42 offen offset:0 nt lds\n\t"
    "v_mfma_f32_16x16x32_bf16 v[152:155], a[72:75], v[56:59], v[152:155]\n\t"
    "v_exp_f32 v247, v141\n\t"
    "v_mfma_f32_16x16x32_bf16 v[152:155], a[76:79], v[60:63], v[152:155]\n\t"
    "ds_read_b128 a[68:71], v229 offset:16384\n\t"
    "v_cvt_pk_bf16_f32 v182, v246, v247\n\t"
    "v_mfma_f32_16x16x32_bf16 v[148:151], a[80:83], v[32:35], v[168:171]\n\t"
    "v_exp_f32 v248, v142\n\t"
    "v_mfma_f32_16x16x32_bf16 v[148:151], a[84:87], v[36:39], v[148:151]\n\t"
    "ds_read_b128 a[72:75], v230 offset:16384\n\t"
    "ds_read_b128 a[76:79], v231 offset:16384\n\t"
    "v_mfma_f32_16x16x32_bf16 v[148:151], a[88:91], v[40:43], v[148:151]\n\t"
    "v_exp_f32 v249, v143\n\t"
    "v_mfma_f32_16x16x32_bf16 v[148:151], a[92:95], v[44:47], v[148:151]\n\t"
    "v_cvt_pk_bf16_f32 v183, v248, v249\n\t"
    "v_add_f32 v253, v253, v220\n\t"
    "v_mfma_f32_16x16x32_bf16 v[156:159], a[80:83], v[48:51], v[172:175]\n\t"
    "v_exp_f32 v246, v144\n\t"
    "v_mfma_f32_16x16x32_bf16 v[212:215], v[224:227], v[180:183], 0\n\t"
    "ds_read_b128 a[80:83], v228 offset:20480\n\t"
    "v_mfma_f32_16x16x32_bf16 v[156:159], a[84:87], v[52:55], v[156:159]\n\t"
    "buffer_load_dwordx4 v241, s[28:31], s42 offen offset:1024 nt lds\n\t"
    "v_mfma_f32_16x16x32_bf16 v[156:159], a[88:91], v[56:59], v[156:159]\n\t"
    "v_exp_f32 v247, v145\n\t"
    "v_mfma_f32_16x16x32_bf16 v[156:159], a[92:95], v[60:63], v[156:159]\n\t"
    "ds_read_b128 a[84:87], v229 offset:20480\n\t"
    "v_cvt_pk_bf16_f32 v184, v246, v247\n\t"
    "v_mfma_f32_16x16x32_bf16 v[64:67], a[96:99], v[200:203], v[64:67]\n\t"
    "v_exp_f32 v248, v146\n\t"
    "v_mfma_f32_16x16x32_bf16 v[68:71], a[96:99], v[204:207], v[68:71]\n\t"
    "v_max_f32 v254, v208, v212\n\t"
    "v_cmp_lt_f32 vcc, s45, v254\n\t"
    "v_mfma_f32_16x16x32_bf16 v[72:75], a[100:103], v[200:203], v[72:75]\n\t"
    "v_exp_f32 v249, v147\n\t"
    "v_mfma_f32_16x16x32_bf16 v[76:79], a[100:103], v[204:207], v[76:79]\n\t"
    "ds_read_b128 a[88:91], v230 offset:20480\n\t"
    "v_cvt_pk_bf16_f32 v185, v248, v249\n\t"
    "v_mfma_f32_16x16x32_bf16 v[80:83], a[104:107], v[200:203], v[80:83]\n\t"
    "v_exp_f32 v246, v148\n\t"
    "v_mfma_f32_16x16x32_bf16 v[84:87], a[104:107], v[204:207], v[84:87]\n\t"
    "buffer_load_dwordx4 v242, s[28:31], s42 offen offset:2048 nt lds\n\t"
    "v_mfma_f32_16x16x32_bf16 v[88:91], a[108:111], v[200:203], v[88:91]\n\t"
    "v_exp_f32 v247, v149\n\t"
    "v_mfma_f32_16x16x32_bf16 v[92:95], a[108:111], v[204:207], v[92:95]\n\t"
    "ds_read_b128 a[92:95], v231 offset:20480\n\t"
    "v_cvt_pk_bf16_f32 v186, v246, v247\n\t"
    "v_mfma_f32_16x16x32_bf16 v[96:99], a[112:115], v[200:203], v[96:99]\n\t"
    "v_exp_f32 v248, v150\n\t"
    "v_mfma_f32_16x16x32_bf16 v[100:103], a[112:115], v[204:207], v[100:103]\n\t"
    "s_cbranch_vccnz .Ltsv_stub9\n\t"
    ".Ltsv_back9:\n\t"
    "v_mfma_f32_16x16x32_bf16 v[104:107], a[116:119], v[200:203], v[104:107]\n\t"
    "v_exp_f32 v249, v151\n\t"
    "v_mfma_f32_16x16x32_bf16 v[108:111], a[116:119], v[204:207], v[108:111]\n\t"
    "ds_read_b64_tr_b16 a[96:97], v232 offset:0\n\t"
    "v_cvt_pk_bf16_f32 v187, v248, v249\n\t"
    "v_mfma_f32_16x16x32_bf16 v[112:115], a[120:123], v[200:203], v[112:115]\n\t"
    "v_exp_f32 v246, v152\n\t"
    "v_mfma_f32_16x16x32_bf16 v[216:219], v[224:227], v[184:187], 0\n\t"
    "ds_read_b64_tr_b16 a[98:99], v232 offset:4096\n\t"
    "ds_read_b64_tr_b16 a[100:101], v233 offset:0\n\t"
    "v_mfma_f32_16x16x32_bf16 v[116:119], a[120:123], v[204:207], v[116:119]\n\t"
    "buffer_load_dwordx4 v243, s[28:31], s42 offen offset:3072 nt lds\n\t"
    "v_mfma_f32_16x16x32_bf16 v[120:123], a[124:127], v[200:203], v[120:123]\n\t"
    "v_exp_f32 v247, v153\n\t"
    "v_mfma_f32_16x16x32_bf16 v[124:127], a[124:127], v[204:207], v[124:127]\n\t"
    "s_waitcnt lgkmcnt(3)\n\t"
    "ds_read_b64_tr_b16 a[102:103], v233 offset:4096\n\t"
    "v_cvt_pk_bf16_f32 v188, v246, v247\n\t"
    "v_mfma_f32_16x16x32_bf16 v[128:131], a[64:67], v[0:3], v[160:163]\n\t"
    "v_exp_f32 v248, v154\n\t"
    "v_mfma_f32_16x16x32_bf16 v[128:131], a[68:71], v[4:7], v[128:131]\n\t"
    "ds_read_b64_tr_b16 a[104:105], v234 offset:0\n\t"
    "ds_read_b64_tr_b16 a[106:107], v234 offset:4096\n\t"
    "v_mfma_f32_16x16x32_bf16 v[128:131], a[72:75], v[8:11], v[128:131]\n\t"
    "s_add_u32 s43, s43, 16384\n\t"
    "v_exp_f32 v249, v155\n\t"
    "v_mfma_f32_16x16x32_bf16 v[128:131], a[76:79], v[12:15], v[128:131]\n\t"
    "ds_read_b64_tr_b16 a[108:109], v235 offset:0\n\t"
    "v_cvt_pk_bf16_f32 v189, v248, v249\n\t"
    "v_mfma_f32_16x16x32_bf16 v[136:139], a[64:67], v[16:19], v[164:167]\n\t"
    "s_add_u32 m0, s41, 49152\n\t"
    "v_exp_f32 v246, v156\n\t"
    "v_mfma_f32_16x16x32_bf16 v[136:139], a[68:71], v[20:23], v[136:139]\n\t"
    "buffer_load_dwordx4 v244, s[32:35], s43 offen offset:0 nt lds\n\t"
    "v_mfma_f32_16x16x32_bf16 v[136:139], a[72:75], v[24:27], v[136:139]\n\t"
    "v_exp_f32 v247, v157\n\t"
    "v_mfma_f32_16x16x32_bf16 v[136:139], a[76:79], v[28:31], v[136:139]\n\t"
    "ds_read_b64_tr_b16 a[110:111], v235 offset:4096\n\t"
    "v_cvt_pk_bf16_f32 v190, v246, v247\n\t"
    "v_mfma_f32_16x16x32_bf16 v[132:135], a[80:83], v[0:3], v[160:163]\n\t"
    "v_exp_f32 v248, v158\n\t"
    "v_mfma_f32_16x16x32_bf16 v[132:135], a[84:87], v[4:7], v[132:135]\n\t"
    "ds_read_b64_tr_b16 a[112:113], v236 offset:0\n\t"
    "ds_read_b64_tr_b16 a[114:115], v236 offset:4096\n\t"
    "v_mfma_f32_16x16x32_bf16 v[132:135], a[88:91], v[8:11], v[132:135]\n\t"
    "v_exp_f32 v249, v159\n\t"
    "v_mfma_f32_16x16x32_bf16 v[132:135], a[92:95], v[12:15], v[132:135]\n\t"
    "ds_read_b64_tr_b16 a[116:117], v237 offset:0\n\t"
    "v_cvt_pk_bf16_f32 v191, v248, v249\n\t"
    "v_mfma_f32_16x16x32_bf16 v[140:143], a[80:83], v[16:19], v[164:167]\n\t"
    "v_exp_f32 v246, v128\n\t"
    "v_mfma_f32_16x16x32_bf16 v[220:223], v[224:227], v[188:191], 0\n\t"
    "ds_read_b64_tr_b16 a[118:119], v237 offset:4096\n\t"
    "ds_read_b64_tr_b16 a[120:121], v238 offset:0\n\t"
    "v_mfma_f32_16x16x32_bf16 v[140:143], a[84:87], v[20:23], v[140:143]\n\t"
    "buffer_load_dwordx4 v245, s[32:35], s43 offen offset:1024 nt lds\n\t"
    "v_mfma_f32_16x16x32_bf16 v[140:143], a[88:91], v[24:27], v[140:143]\n\t"
    "v_exp_f32 v247, v129\n\t"
    "v_mfma_f32_16x16x32_bf16 v[140:143], a[92:95], v[28:31], v[140:143]\n\t"
    "s_waitcnt lgkmcnt(5)\n\t"
    "ds_read_b64_tr_b16 a[122:123], v238 offset:4096\n\t"
    "v_cvt_pk_bf16_f32 v192, v246, v247\n\t"
    "v_mfma_f32_16x16x32_bf16 a[0:3], a[96:99], v[176:179], a[0:3]\n\t"
    "v_exp_f32 v248, v130\n\t"
    "v_mfma_f32_16x16x32_bf16 a[4:7], a[96:99], v[180:183], a[4:7]\n\t"
    "v_max_f32 v254, v216, v220\n\t"
    "v_cmp_lt_f32 vcc, s45, v254\n\t"
    "v_mfma_f32_16x16x32_bf16 a[8:11], a[100:103], v[176:179], a[8:11]\n\t"
    "v_exp_f32 v249, v131\n\t"
    "v_mfma_f32_16x16x32_bf16 a[12:15], a[100:103], v[180:183], a[12:15]\n\t"
    "ds_read_b64_tr_b16 a[124:125], v239 offset:0\n\t"
    "v_cvt_pk_bf16_f32 v193, v248, v249\n\t"
    "v_mfma_f32_16x16x32_bf16 a[16:19], a[104:107], v[176:179], a[16:19]\n\t"
    "v_exp_f32 v246, v132\n\t"
    "v_mfma_f32_16x16x32_bf16 a[20:23], a[104:107], v[180:183], a[20:23]\n\t"
    "buffer_load_dwordx4 v244, s[32:35], s43 offen offset:2048 nt lds\n\t"
    "v_mfma_f32_16x16x32_bf16 a[24:27], a[108:111], v[176:179], a[24:27]\n\t"
    "v_exp_f32 v247, v133\n\t"
    "v_mfma_f32_16x16x32_bf16 a[28:31], a[108:111], v[180:183], a[28:31]\n\t"
    "s_waitcnt lgkmcnt(1)\n\t"
    "ds_read_b64_tr_b16 a[126:127], v239 offset:4096\n\t"
    "v_cvt_pk_bf16_f32 v194, v246, v247\n\t"
    "v_mfma_f32_16x16x32_bf16 a[32:35], a[112:115], v[176:179], a[32:35]\n\t"
    "v_exp_f32 v248, v134\n\t"
    "v_mfma_f32_16x16x32_bf16 a[36:39], a[112:115], v[180:183], a[36:39]\n\t"
    "s_cbranch_vccnz .Ltsv_stub10\n\t"
    ".Ltsv_back10:\n\t"
    "v_mfma_f32_16x16x32_bf16 a[40:43], a[116:119], v[176:179], a[40:43]\n\t"
    "v_exp_f32 v249, v135\n\t"
    "v_mfma_f32_16x16x32_bf16 a[44:47], a[116:119], v[180:183], a[44:47]\n\t"
    "v_cvt_pk_bf16_f32 v195, v248, v249\n\t"
    "v_add_f32 v250, v250, v208\n\t"
    "v_mfma_f32_16x16x32_bf16 a[48:51], a[120:123], v[176:179], a[48:51]\n\t"
    "v_exp_f32 v246, v136\n\t"
    "v_mfma_f32_16x16x32_bf16 a[52:55], a[120:123], v[180:183], a[52:55]\n\t"
    "s_waitcnt lgkmcnt(0)\n\t"
    "buffer_load_dwordx4 v245, s[32:35], s43 offen offset:3072 nt lds\n\t"
    "v_mfma_f32_16x16x32_bf16 a[56:59], a[124:127], v[176:179], a[56:59]\n\t"
    "v_exp_f32 v247, v137\n\t"
    "v_mfma_f32_16x16x32_bf16 a[60:63], a[124:127], v[180:183], a[60:63]\n\t"
    "v_cvt_pk_bf16_f32 v196, v246, v247\n\t"
    "v_add_f32 v251, v251, v212\n\t"
    "v_mfma_f32_16x16x32_bf16 v[144:147], a[64:67], v[32:35], v[168:171]\n\t"
    "s_cmp_gt_i32 s94, 0\n\t"
    "v_exp_f32 v248, v138\n\t"
    "v_mfma_f32_16x16x32_bf16 v[144:147], a[68:71], v[36:39], v[144:147]\n\t"
    "s_cbranch_scc1 .Ltsv_dly11\n\t"
    ".Ltsv_back11:\n\t"
    "s_waitcnt vmcnt(16)\n\t"
    "s_barrier\n\t"
    "v_mfma_f32_16x16x32_bf16 v[144:147], a[72:75], v[40:43], v[144:147]\n\t"
    "s_add_u32 s42, s42, 16384\n\t"
    "v_exp_f32 v249, v139\n\t"
    "v_mfma_f32_16x16x32_bf16 v[144:147], a[76:79], v[44:47], v[144:147]\n\t"
    "v_cvt_pk_bf16_f32 v197, v248, v249\n\t"
    "v_add_f32 v252, v252, v216\n\t"
    "v_mfma_f32_16x16x32_bf16 v[152:155], a[64:67], v[48:51], v[172:175]\n\t"
    "s_add_u32 m0, s40, 16384\n\t"
    "v_exp_f32 v246, v140\n\t"
    "v_mfma_f32_16x16x32_bf16 v[208:211], v[224:227], v[192:195], 0\n\t"
    "ds_read_b128 a[64:67], v228 offset:32768\n\t"
    "v_mfma_f32_16x16x32_bf16 v[152:155], a[68:71], v[52:55], v[152:155]\n\t"
    "buffer_load_dwordx4 v240, s[28:31], s42 offen offset:0 nt lds\n\t"
    "v_mfma_f32_16x16x32_bf16 v[152:155], a[72:75], v[56:59], v[152:155]\n\t"
    "v_exp_f32 v247, v141\n\t"
    "v_mfma_f32_16x16x32_bf16 v[152:155], a[76:79], v[60:63], v[152:155]\n\t"
    "ds_read_b128 a[68:71], v229 offset:32768\n\t"
    "v_cvt_pk_bf16_f32 v198, v246, v247\n\t"
    "v_mfma_f32_16x16x32_bf16 v[148:151], a[80:83], v[32:35], v[168:171]\n\t"
    "v_exp_f32 v248, v142\n\t"
    "v_mfma_f32_16x16x32_bf16 v[148:151], a[84:87], v[36:39], v[148:151]\n\t"
    "ds_read_b128 a[72:75], v230 offset:32768\n\t"
    "ds_read_b128 a[76:79], v231 offset:32768\n\t"
    "v_mfma_f32_16x16x32_bf16 v[148:151], a[88:91], v[40:43], v[148:151]\n\t"
    "v_exp_f32 v249, v143\n\t"
    "v_mfma_f32_16x16x32_bf16 v[148:151], a[92:95], v[44:47], v[148:151]\n\t"
    "v_cvt_pk_bf16_f32 v199, v248, v249\n\t"
    "v_add_f32 v253, v253, v220\n\t"
    "v_mfma_f32_16x16x32_bf16 v[156:159], a[80:83], v[48:51], v[172:175]\n\t"
    "v_exp_f32 v246, v144\n\t"
    "v_mfma_f32_16x16x32_bf16 v[212:215], v[224:227], v[196:199], 0\n\t"
    "ds_read_b128 a[80:83], v228 offset:36864\n\t"
    "v_mfma_f32_16x16x32_bf16 v[156:159], a[84:87], v[52:55], v[156:159]\n\t"
    "buffer_load_dwordx4 v241, s[28:31], s42 offen offset:1024 nt lds\n\t"
    "v_mfma_f32_16x16x32_bf16 v[156:159], a[88:91], v[56:59], v[156:159]\n\t"
    "v_exp_f32 v247, v145\n\t"
    "v_mfma_f32_16x16x32_bf16 v[156:159], a[92:95], v[60:63], v[156:159]\n\t"
    "ds_read_b128 a[84:87], v229 offset:36864\n\t"
    "v_cvt_pk_bf16_f32 v200, v246, v247\n\t"
    "v_mfma_f32_16x16x32_bf16 v[64:67], a[96:99], v[184:187], v[64:67]\n\t"
    "v_exp_f32 v248, v146\n\t"
    "v_mfma_f32_16x16x32_bf16 v[68:71], a[96:99], v[188:191], v[68:71]\n\t"
    "v_max_f32 v254, v208, v212\n\t"
    "v_cmp_lt_f32 vcc, s45, v254\n\t"
    "v_mfma_f32_16x16x32_bf16 v[72:75], a[100:103], v[184:187], v[72:75]\n\t"
    "v_exp_f32 v249, v147\n\t"
    "v_mfma_f32_16x16x32_bf16 v[76:79], a[100:103], v[188:191], v[76:79]\n\t"
    "ds_read_b128 a[88:91], v230 offset:36864\n\t"
    "v_cvt_pk_bf16_f32 v201, v248, v249\n\t"
    "v_mfma_f32_16x16x32_bf16 v[80:83], a[104:107], v[184:187], v[80:83]\n\t"
    "v_exp_f32 v246, v148\n\t"
    "v_mfma_f32_16x16x32_bf16 v[84:87], a[104:107], v[188:191], v[84:87]\n\t"
    "buffer_load_dwordx4 v242, s[28:31], s42 offen offset:2048 nt lds\n\t"
    "v_mfma_f32_16x16x32_bf16 v[88:91], a[108:111], v[184:187], v[88:91]\n\t"
    "v_exp_f32 v247, v149\n\t"
    "v_mfma_f32_16x16x32_bf16 v[92:95], a[108:111], v[188:191], v[92:95]\n\t"
    "ds_read_b128 a[92:95], v231 offset:36864\n\t"
    "v_cvt_pk_bf16_f32 v202, v246, v247\n\t"
    "v_mfma_f32_16x16x32_bf16 v[96:99], a[112:115], v[184:187], v[96:99]\n\t"
    "v_exp_f32 v248, v150\n\t"
    "v_mfma_f32_16x16x32_bf16 v[100:103], a[112:115], v[188:191], v[100:103]\n\t"
    "s_cbranch_vccnz .Ltsv_stub12\n\t"
    ".Ltsv_back12:\n\t"
    "v_mfma_f32_16x16x32_bf16 v[104:107], a[116:119], v[184:187], v[104:107]\n\t"
    "v_exp_f32 v249, v151\n\t"
    "v_mfma_f32_16x16x32_bf16 v[108:111], a[116:119], v[188:191], v[108:111]\n\t"
    "ds_read_b64_tr_b16 a[96:97], v232 offset:16384\n\t"
    "v_cvt_pk_bf16_f32 v203, v248, v249\n\t"
    "v_mfma_f32_16x16x32_bf16 v[112:115], a[120:123], v[184:187], v[112:115]\n\t"
    "s_sub_u32 s44, s44, 1\n\t"
    "v_exp_f32 v246, v152\n\t"
    "v_mfma_f32_16x16x32_bf16 v[216:219], v[224:227], v[200:203], 0\n\t"
    "ds_read_b64_tr_b16 a[98:99], v232 offset:20480\n\t"
    "ds_read_b64_tr_b16 a[100:101], v233 offset:16384\n\t"
    "v_mfma_f32_16x16x32_bf16 v[116:119], a[120:123], v[188:191], v[116:119]\n\t"
    "buffer_load_dwordx4 v243, s[28:31], s42 offen offset:3072 nt lds\n\t"
    "v_mfma_f32_16x16x32_bf16 v[120:123], a[124:127], v[184:187], v[120:123]\n\t"
    "s_cmp_lg_u32 s44, 0\n\t"
    "v_exp_f32 v247, v153\n\t"
    "v_mfma_f32_16x16x32_bf16 v[124:127], a[124:127], v[188:191], v[124:127]\n\t"
    "s_waitcnt lgkmcnt(3)\n\t"
    "ds_read_b64_tr_b16 a[102:103], v233 offset:20480\n\t"
    "v_cvt_pk_bf16_f32 v204, v246, v247\n\t"
    "s_cbranch_scc1 .Ltsv_loop\n\t"
    "v_mfma_f32_16x16x32_bf16 v[128:131], a[64:67], v[0:3], v[160:163]\n\t"
    "v_exp_f32 v248, v154\n\t"
    "v_mfma_f32_16x16x32_bf16 v[128:131], a[68:71], v[4:7], v[128:131]\n\t"
    "ds_read_b64_tr_b16 a[104:105], v234 offset:16384\n\t"
    "ds_read_b64_tr_b16 a[106:107], v234 offset:20480\n\t"
    "v_mfma_f32_16x16x32_bf16 v[128:131], a[72:75], v[8:11], v[128:131]\n\t"
    "v_exp_f32 v249, v155\n\t"
    "v_mfma_f32_16x16x32_bf16 v[128:131], a[76:79], v[12:15], v[128:131]\n\t"
    "ds_read_b64_tr_b16 a[108:109], v235 offset:16384\n\t"
    "v_cvt_pk_bf16_f32 v205, v248, v249\n\t"
    "v_mfma_f32_16x16x32_bf16 v[136:139], a[64:67], v[16:19], v[164:167]\n\t"
    "v_exp_f32 v246, v156\n\t"
    "v_mfma_f32_16x16x32_bf16 v[136:139], a[68:71], v[20:23], v[136:139]\n\t"
    "v_mfma_f32_16x16x32_bf16 v[136:139], a[72:75], v[24:27], v[136:139]\n\t"
    "v_exp_f32 v247, v157\n\t"
    "v_mfma_f32_16x16x32_bf16 v[136:139], a[76:79], v[28:31], v[136:139]\n\t"
    "ds_read_b64_tr_b16 a[110:111], v235 offset:20480\n\t"
    "v_cvt_pk_bf16_f32 v206, v246, v247\n\t"
    "v_mfma_f32_16x16x32_bf16 v[132:135], a[80:83], v[0:3], v[160:163]\n\t"
    "v_exp_f32 v248, v158\n\t"
    "v_mfma_f32_16x16x32_bf16 v[132:135], a[84:87], v[4:7], v[132:135]\n\t"
    "ds_read_b64_tr_b16 a[112:113], v236 offset:16384\n\t"
    "ds_read_b64_tr_b16 a[114:115], v236 offset:20480\n\t"
    "v_mfma_f32_16x16x32_bf16 v[132:135], a[88:91], v[8:11], v[132:135]\n\t"
    "v_exp_f32 v249, v159\n\t"
    "v_mfma_f32_16x16x32_bf16 v[132:135], a[92:95], v[12:15], v[132:135]\n\t"
    "ds_read_b64_tr_b16 a[116:117], v237 offset:16384\n\t"
    "v_cvt_pk_bf16_f32 v207, v248, v249\n\t"
    "v_mfma_f32_16x16x32_bf16 v[140:143], a[80:83], v[16:19], v[164:167]\n\t"
    "v_exp_f32 v246, v128\n\t"
    "v_mfma_f32_16x16x32_bf16 v[220:223], v[224:227], v[204:207], 0\n\t"
    "ds_read_b64_tr_b16 a[118:119], v237 offset:20480\n\t"
    "ds_read_b64_tr_b16 a[120:121], v238 offset:16384\n\t"
    "v_mfma_f32_16x16x32_bf16 v[140:143], a[84:87], v[20:23], v[140:143]\n\t"
    "v_mfma_f32_16x16x32_bf16 v[140:143], a[88:91], v[24:27], v[140:143]\n\t"
    "v_exp_f32 v247, v129\n\t"
    "v_mfma_f32_16x16x32_bf16 v[140:143], a[92:95], v[28:31], v[140:143]\n\t"
    "s_waitcnt lgkmcnt(5)\n\t"
    "ds_read_b64_tr_b16 a[122:123], v238 offset:20480\n\t"
    "v_cvt_pk_bf16_f32 v176, v246, v247\n\t"
    "v_mfma_f32_16x16x32_bf16 a[0:3], a[96:99], v[192:195], a[0:3]\n\t"
    "v_exp_f32 v248, v130\n\t"
    "v_mfma_f32_16x16x32_bf16 a[4:7], a[96:99], v[196:199], a[4:7]\n\t"
    "v_max_f32 v254, v216, v220\n\t"
    "v_cmp_lt_f32 vcc, s45, v254\n\t"
    "v_mfma_f32_16x16x32_bf16 a[8:11], a[100:103], v[192:195], a[8:11]\n\t"
    "v_exp_f32 v249, v131\n\t"
    "v_mfma_f32_16x16x32_bf16 a[12:15], a[100:103], v[196:199], a[12:15]\n\t"
    "ds_read_b64_tr_b16 a[124:125], v239 offset:16384\n\t"
    "v_cvt_pk_bf16_f32 v177, v248, v249\n\t"
    "v_mfma_f32_16x16x32_bf16 a[16:19], a[104:107], v[192:195], a[16:19]\n\t"
    "v_exp_f32 v246, v132\n\t"
    "v_mfma_f32_16x16x32_bf16 a[20:23], a[104:107], v[196:199], a[20:23]\n\t"
    "v_mfma_f32_16x16x32_bf16 a[24:27], a[108:111], v[192:195], a[24:27]\n\t"
    "v_exp_f32 v247, v133\n\t"
    "v_mfma_f32_16x16x32_bf16 a[28:31], a[108:111], v[196:199], a[28:31]\n\t"
    "s_waitcnt lgkmcnt(1)\n\t"
    "ds_read_b64_tr_b16 a[126:127], v239 offset:20480\n\t"
    "v_cvt_pk_bf16_f32 v178, v246, v247\n\t"
    "v_mfma_f32_16x16x32_bf16 a[32:35], a[112:115], v[192:195], a[32:35]\n\t"
    "v_exp_f32 v248, v134\n\t"
    "v_mfma_f32_16x16x32_bf16 a[36:39], a[112:115], v[196:199], a[36:39]\n\t"
    "s_cbranch_vccnz .Ltsv_stub13\n\t"
    ".Ltsv_back13:\n\t"
    "v_mfma_f32_16x16x32_bf16 a[40:43], a[116:119], v[192:195], a[40:43]\n\t"
    "v_exp_f32 v249, v135\n\t"
    "v_mfma_f32_16x16x32_bf16 a[44:47], a[116:119], v[196:199], a[44:47]\n\t"
    "v_cvt_pk_bf16_f32 v179, v248, v249\n\t"
    "v_add_f32 v250, v250, v208\n\t"
    "v_mfma_f32_16x16x32_bf16 a[48:51], a[120:123], v[192:195], a[48:51]\n\t"
    "v_exp_f32 v246, v136\n\t"
    "v_mfma_f32_16x16x32_bf16 a[52:55], a[120:123], v[196:199], a[52:55]\n\t"
    "s_waitcnt lgkmcnt(0)\n\t"
    "v_mfma_f32_16x16x32_bf16 a[56:59], a[124:127], v[192:195], a[56:59]\n\t"
    "v_exp_f32 v247, v137\n\t"
    "v_mfma_f32_16x16x32_bf16 a[60:63], a[124:127], v[196:199], a[60:63]\n\t"
    "v_cvt_pk_bf16_f32 v180, v246, v247\n\t"
    "v_add_f32 v251, v251, v212\n\t"
    "v_mfma_f32_16x16x32_bf16 v[144:147], a[64:67], v[32:35], v[168:171]\n\t"
    "v_exp_f32 v248, v138\n\t"
    "v_mfma_f32_16x16x32_bf16 v[144:147], a[68:71], v[36:39], v[144:147]\n\t"
    "s_waitcnt vmcnt(12)\n\t"
    "s_barrier\n\t"
    "v_mfma_f32_16x16x32_bf16 v[144:147], a[72:75], v[40:43], v[144:147]\n\t"
    "v_exp_f32 v249, v139\n\t"
    "v_mfma_f32_16x16x32_bf16 v[144:147], a[76:79], v[44:47], v[144:147]\n\t"
    "v_cvt_pk_bf16_f32 v181, v248, v249\n\t"
    "v_add_f32 v252, v252, v216\n\t"
    "v_mfma_f32_16x16x32_bf16 v[152:155], a[64:67], v[48:51], v[172:175]\n\t"
    "v_exp_f32 v246, v140\n\t"
    "v_mfma_f32_16x16x32_bf16 v[208:211], v[224:227], v[176:179], 0\n\t"
    "ds_read_b128 a[64:67], v228 offset:49152\n\t"
    "v_mfma_f32_16x16x32_bf16 v[152:155], a[68:71], v[52:55], v[152:155]\n\t"
    "v_mfma_f32_16x16x32_bf16 v[152:155], a[72:75], v[56:59], v[152:155]\n\t"
    "v_exp_f32 v247, v141\n\t"
    "v_mfma_f32_16x16x32_bf16 v[152:155], a[76:79], v[60:63], v[152:155]\n\t"
    "ds_read_b128 a[68:71], v229 offset:49152\n\t"
    "v_cvt_pk_bf16_f32 v182, v246, v247\n\t"
    "v_mfma_f32_16x16x32_bf16 v[148:151], a[80:83], v[32:35], v[168:171]\n\t"
    "v_exp_f32 v248, v142\n\t"
    "v_mfma_f32_16x16x32_bf16 v[148:151], a[84:87], v[36:39], v[148:151]\n\t"
    "ds_read_b128 a[72:75], v230 offset:49152\n\t"
    "ds_read_b128 a[76:79], v231 offset:49152\n\t"
    "v_mfma_f32_16x16x32_bf16 v[148:151], a[88:91], v[40:43], v[148:151]\n\t"
    "v_exp_f32 v249, v143\n\t"
    "v_mfma_f32_16x16x32_bf16 v[148:151], a[92:95], v[44:47], v[148:151]\n\t"
    "v_cvt_pk_bf16_f32 v183, v248, v249\n\t"
    "v_add_f32 v253, v253, v220\n\t"
    "v_mfma_f32_16x16x32_bf16 v[156:159], a[80:83], v[48:51], v[172:175]\n\t"
    "v_exp_f32 v246, v144\n\t"
    "v_mfma_f32_16x16x32_bf16 v[212:215], v[224:227], v[180:183], 0\n\t"
    "ds_read_b128 a[80:83], v228 offset:53248\n\t"
    "v_mfma_f32_16x16x32_bf16 v[156:159], a[84:87], v[52:55], v[156:159]\n\t"
    "v_mfma_f32_16x16x32_bf16 v[156:159], a[88:91], v[56:59], v[156:159]\n\t"
    "v_exp_f32 v247, v145\n\t"
    "v_mfma_f32_16x16x32_bf16 v[156:159], a[92:95], v[60:63], v[156:159]\n\t"
    "ds_read_b128 a[84:87], v229 offset:53248\n\t"
    "v_cvt_pk_bf16_f32 v184, v246, v247\n\t"
    "v_mfma_f32_16x16x32_bf16 v[64:67], a[96:99], v[200:203], v[64:67]\n\t"
    "v_exp_f32 v248, v146\n\t"
    "v_mfma_f32_16x16x32_bf16 v[68:71], a[96:99], v[204:207], v[68:71]\n\t"
    "v_max_f32 v254, v208, v212\n\t"
    "v_cmp_lt_f32 vcc, s45, v254\n\t"
    "v_mfma_f32_16x16x32_bf16 v[72:75], a[100:103], v[200:203], v[72:75]\n\t"
    "v_exp_f32 v249, v147\n\t"
    "v_mfma_f32_16x16x32_bf16 v[76:79], a[100:103], v[204:207], v[76:79]\n\t"
    "ds_read_b128 a[88:91], v230 offset:53248\n\t"
    "v_cvt_pk_bf16_f32 v185, v248, v249\n\t"
    "v_mfma_f32_16x16x32_bf16 v[80:83], a[104:107], v[200:203], v[80:83]\n\t"
    "v_exp_f32 v246, v148\n\t"
    "v_mfma_f32_16x16x32_bf16 v[84:87], a[104:107], v[204:207], v[84:87]\n\t"
    "v_mfma_f32_16x16x32_bf16 v[88:91], a[108:111], v[200:203], v[88:91]\n\t"
    "v_exp_f32 v247, v149\n\t"
    "v_mfma_f32_16x16x32_bf16 v[92:95], a[108:111], v[204:207], v[92:95]\n\t"
    "ds_read_b128 a[92:95], v231 offset:53248\n\t"
    "v_cvt_pk_bf16_f32 v186, v246, v247\n\t"
    "v_mfma_f32_16x16x32_bf16 v[96:99], a[112:115], v[200:203], v[96:99]\n\t"
    "v_exp_f32 v248, v150\n\t"
    "v_mfma_f32_16x16x32_bf16 v[100:103], a[112:115], v[204:207], v[100:103]\n\t"
    "s_cbranch_vccnz .Ltsv_stub14\n\t"
    ".Ltsv_back14:\n\t"
    "v_mfma_f32_16x16x32_bf16 v[104:107], a[116:119], v[200:203], v[104:107]\n\t"
    "v_exp_f32 v249, v151\n\t"
    "v_mfma_f32_16x16x32_bf16 v[108:111], a[116:119], v[204:207], v[108:111]\n\t"
    "ds_read_b64_tr_b16 a[96:97], v232 offset:32768\n\t"
    "v_cvt_pk_bf16_f32 v187, v248, v249\n\t"
    "v_mfma_f32_16x16x32_bf16 v[112:115], a[120:123], v[200:203], v[112:115]\n\t"
    "v_exp_f32 v246, v152\n\t"
    "v_mfma_f32_16x16x32_bf16 v[216:219], v[224:227], v[184:187], 0\n\t"
    "ds_read_b64_tr_b16 a[98:99], v232 offset:36864\n\t"
    "ds_read_b64_tr_b16 a[100:101], v233 offset:32768\n\t"
    "v_mfma_f32_16x16x32_bf16 v[116:119], a[120:123], v[204:207], v[116:119]\n\t"
    "v_mfma_f32_16x16x32_bf16 v[120:123], a[124:127], v[200:203], v[120:123]\n\t"
    "v_exp_f32 v247, v153\n\t"
    "v_mfma_f32_16x16x32_bf16 v[124:127], a[124:127], v[204:207], v[124:127]\n\t"
    "s_waitcnt lgkmcnt(3)\n\t"
    "ds_read_b64_tr_b16 a[102:103], v233 offset:36864\n\t"
    "v_cvt_pk_bf16_f32 v188, v246, v247\n\t"
    "v_mfma_f32_16x16x32_bf16 v[128:131], a[64:67], v[0:3], v[160:163]\n\t"
    "v_exp_f32 v248, v154\n\t"
    "v_mfma_f32_16x16x32_bf16 v[128:131], a[68:71], v[4:7], v[128:131]\n\t"
    "ds_read_b64_tr_b16 a[104:105], v234 offset:32768\n\t"
    "ds_read_b64_tr_b16 a[106:107], v234 offset:36864\n\t"
    "v_mfma_f32_16x16x32_bf16 v[128:131], a[72:75], v[8:11], v[128:131]\n\t"
    "v_exp_f32 v249, v155\n\t"
    "v_mfma_f32_16x16x32_bf16 v[128:131], a[76:79], v[12:15], v[128:131]\n\t"
    "ds_read_b64_tr_b16 a[108:109], v235 offset:32768\n\t"
    "v_cvt_pk_bf16_f32 v189, v248, v249\n\t"
    "v_mfma_f32_16x16x32_bf16 v[136:139], a[64:67], v[16:19], v[164:167]\n\t"
    "v_exp_f32 v246, v156\n\t"
    "v_mfma_f32_16x16x32_bf16 v[136:139], a[68:71], v[20:23], v[136:139]\n\t"
    "v_mfma_f32_16x16x32_bf16 v[136:139], a[72:75], v[24:27], v[136:139]\n\t"
    "v_exp_f32 v247, v157\n\t"
    "v_mfma_f32_16x16x32_bf16 v[136:139], a[76:79], v[28:31], v[136:139]\n\t"
    "ds_read_b64_tr_b16 a[110:111], v235 offset:36864\n\t"
    "v_cvt_pk_bf16_f32 v190, v246, v247\n\t"
    "v_mfma_f32_16x16x32_bf16 v[132:135], a[80:83], v[0:3], v[160:163]\n\t"
    "v_exp_f32 v248, v158\n\t"
    "v_mfma_f32_16x16x32_bf16 v[132:135], a[84:87], v[4:7], v[132:135]\n\t"
    "ds_read_b64_tr_b16 a[112:113], v236 offset:32768\n\t"
    "ds_read_b64_tr_b16 a[114:115], v236 offset:36864\n\t"
    "v_mfma_f32_16x16x32_bf16 v[132:135], a[88:91], v[8:11], v[132:135]\n\t"
    "v_exp_f32 v249, v159\n\t"
    "v_mfma_f32_16x16x32_bf16 v[132:135], a[92:95], v[12:15], v[132:135]\n\t"
    "ds_read_b64_tr_b16 a[116:117], v237 offset:32768\n\t"
    "v_cvt_pk_bf16_f32 v191, v248, v249\n\t"
    "v_mfma_f32_16x16x32_bf16 v[140:143], a[80:83], v[16:19], v[164:167]\n\t"
    "v_exp_f32 v246, v128\n\t"
    "v_mfma_f32_16x16x32_bf16 v[220:223], v[224:227], v[188:191], 0\n\t"
    "ds_read_b64_tr_b16 a[118:119], v237 offset:36864\n\t"
    "ds_read_b64_tr_b16 a[120:121], v238 offset:32768\n\t"
    "v_mfma_f32_16x16x32_bf16 v[140:143], a[84:87], v[20:23], v[140:143]\n\t"
    "v_mfma_f32_16x16x32_bf16 v[140:143], a[88:91], v[24:27], v[140:143]\n\t"
    "v_exp_f32 v247, v129\n\t"
    "v_mfma_f32_16x16x32_bf16 v[140:143], a[92:95], v[28:31], v[140:143]\n\t"
    "s_waitcnt lgkmcnt(5)\n\t"
    "ds_read_b64_tr_b16 a[122:123], v238 offset:36864\n\t"
    "v_cvt_pk_bf16_f32 v192, v246, v247\n\t"
    "v_mfma_f32_16x16x32_bf16 a[0:3], a[96:99], v[176:179], a[0:3]\n\t"
    "v_exp_f32 v248, v130\n\t"
    "v_mfma_f32_16x16x32_bf16 a[4:7], a[96:99], v[180:183], a[4:7]\n\t"
    "v_max_f32 v254, v216, v220\n\t"
    "v_cmp_lt_f32 vcc, s45, v254\n\t"
    "v_mfma_f32_16x16x32_bf16 a[8:11], a[100:103], v[176:179], a[8:11]\n\t"
    "v_exp_f32 v249, v131\n\t"
    "v_mfma_f32_16x16x32_bf16 a[12:15], a[100:103], v[180:183], a[12:15]\n\t"
    "ds_read_b64_tr_b16 a[124:125], v239 offset:32768\n\t"
    "v_cvt_pk_bf16_f32 v193, v248, v249\n\t"
    "v_mfma_f32_16x16x32_bf16 a[16:19], a[104:107], v[176:179], a[16:19]\n\t"
    "v_exp_f32 v246, v132\n\t"
    "v_mfma_f32_16x16x32_bf16 a[20:23], a[104:107], v[180:183], a[20:23]\n\t"
    "s_lshl_b32 s58, s55, 14\n\t"
    "s_lshl_b32 s59, s55, 10\n\t"
    "s_add_u32 s59, s59, 131072\n\t"
    "v_mbcnt_lo_u32_b32 v3, -1, 0\n\t"
    "v_mbcnt_hi_u32_b32 v3, -1, v3\n\t"
    "v_mfma_f32_16x16x32_bf16 a[24:27], a[108:111], v[176:179], a[24:27]\n\t"
    "v_exp_f32 v247, v133\n\t"
    "v_mfma_f32_16x16x32_bf16 a[28:31], a[108:111], v[180:183], a[28:31]\n\t"
    "s_waitcnt lgkmcnt(1)\n\t"
    "ds_read_b64_tr_b16 a[126:127], v239 offset:36864\n\t"
    "v_cvt_pk_bf16_f32 v194, v246, v247\n\t"
    "v_mfma_f32_16x16x32_bf16 a[32:35], a[112:115], v[176:179], a[32:35]\n\t"
    "v_exp_f32 v248, v134\n\t"
    "v_mfma_f32_16x16x32_bf16 a[36:39], a[112:115], v[180:183], a[36:39]\n\t"
    "s_cbranch_vccnz .Ltsv_stub15\n\t"
    ".Ltsv_back15:\n\t"
    "v_mfma_f32_16x16x32_bf16 a[40:43], a[116:119], v[176:179], a[40:43]\n\t"
    "v_exp_f32 v249, v135\n\t"
    "v_mfma_f32_16x16x32_bf16 a[44:47], a[116:119], v[180:183], a[44:47]\n\t"
    "v_cvt_pk_bf16_f32 v195, v248, v249\n\t"
    "v_add_f32 v250, v250, v208\n\t"
    "v_mfma_f32_16x16x32_bf16 a[48:51], a[120:123], v[176:179], a[48:51]\n\t"
    "v_exp_f32 v246, v136\n\t"
    "v_mfma_f32_16x16x32_bf16 a[52:55], a[120:123], v[180:183], a[52:55]\n\t"
    "s_waitcnt lgkmcnt(0)\n\t"
    "v_lshlrev_b32 v0, 4, v3\n\t"
    "v_add_u32 v2, s59, v0\n\t"
    "v_mfma_f32_16x16x32_bf16 a[56:59], a[124:127], v[176:179], a[56:59]\n\t"
    "v_exp_f32 v247, v137\n\t"
    "v_mfma_f32_16x16x32_bf16 a[60:63], a[124:127], v[180:183], a[60:63]\n\t"
    "v_cvt_pk_bf16_f32 v196, v246, v247\n\t"
    "v_add_f32 v251, v251, v212\n\t"
    "v_mfma_f32_16x16x32_bf16 v[144:147], a[64:67], v[32:35], v[168:171]\n\t"
    "v_exp_f32 v248, v138\n\t"
    "v_mfma_f32_16x16x32_bf16 v[144:147], a[68:71], v[36:39], v[144:147]\n\t"
    "s_waitcnt vmcnt(0)\n\t"
    "s_barrier\n\t"
    "v_mfma_f32_16x16x32_bf16 v[144:147], a[72:75], v[40:43], v[144:147]\n\t"
    "v_exp_f32 v249, v139\n\t"
    "v_mfma_f32_16x16x32_bf16 v[144:147], a[76:79], v[44:47], v[144:147]\n\t"
    "v_cvt_pk_bf16_f32 v197, v248, v249\n\t"
    "v_add_f32 v252, v252, v216\n\t"
    "v_mfma_f32_16x16x32_bf16 v[152:155], a[64:67], v[48:51], v[172:175]\n\t"
    "v_exp_f32 v246, v140\n\t"
    "v_mfma_f32_16x16x32_bf16 v[208:211], v[224:227], v[192:195], 0\n\t"
    "ds_read_b64_tr_b16 a[128:129], v232 offset:49152\n\t"
    "v_mfma_f32_16x16x32_bf16 v[152:155], a[68:71], v[52:55], v[152:155]\n\t"
    "ds_read_b64_tr_b16 a[130:131], v232 offset:53248\n\t"
    "ds_read_b64_tr_b16 a[132:133], v233 offset:49152\n\t"
    "v_mfma_f32_16x16x32_bf16 v[152:155], a[72:75], v[56:59], v[152:155]\n\t"
    "v_exp_f32 v247, v141\n\t"
    "v_mfma_f32_16x16x32_bf16 v[152:155], a[76:79], v[60:63], v[152:155]\n\t"
    "ds_read_b64_tr_b16 a[134:135], v233 offset:53248\n\t"
    "v_cvt_pk_bf16_f32 v198, v246, v247\n\t"
    "v_mfma_f32_16x16x32_bf16 v[148:151], a[80:83], v[32:35], v[168:171]\n\t"
    "v_exp_f32 v248, v142\n\t"
    "v_mfma_f32_16x16x32_bf16 v[148:151], a[84:87], v[36:39], v[148:151]\n\t"
    "ds_read_b64_tr_b16 a[136:137], v234 offset:49152\n\t"
    "ds_read_b64_tr_b16 a[138:139], v234 offset:53248\n\t"
    "v_mfma_f32_16x16x32_bf16 v[148:151], a[88:91], v[40:43], v[148:151]\n\t"
    "v_exp_f32 v249, v143\n\t"
    "v_mfma_f32_16x16x32_bf16 v[148:151], a[92:95], v[44:47], v[148:151]\n\t"
    "v_cvt_pk_bf16_f32 v199, v248, v249\n\t"
    "v_add_f32 v253, v253, v220\n\t"
    "v_mfma_f32_16x16x32_bf16 v[156:159], a[80:83], v[48:51], v[172:175]\n\t"
    "v_exp_f32 v246, v144\n\t"
    "v_mfma_f32_16x16x32_bf16 v[212:215], v[224:227], v[196:199], 0\n\t"
    "ds_read_b64_tr_b16 a[140:141], v235 offset:49152\n\t"
    "v_mfma_f32_16x16x32_bf16 v[156:159], a[84:87], v[52:55], v[156:159]\n\t"
    "ds_read_b64_tr_b16 a[142:143], v235 offset:53248\n\t"
    "ds_read_b64_tr_b16 a[144:145], v236 offset:49152\n\t"
    "v_mfma_f32_16x16x32_bf16 v[156:159], a[88:91], v[56:59], v[156:159]\n\t"
    "v_exp_f32 v247, v145\n\t"
    "v_mfma_f32_16x16x32_bf16 v[156:159], a[92:95], v[60:63], v[156:159]\n\t"
    "ds_read_b64_tr_b16 a[146:147], v236 offset:53248\n\t"
    "v_cvt_pk_bf16_f32 v200, v246, v247\n\t"
    "v_mfma_f32_16x16x32_bf16 v[64:67], a[96:99], v[184:187], v[64:67]\n\t"
    "v_exp_f32 v248, v146\n\t"
    "v_mfma_f32_16x16x32_bf16 v[68:71], a[96:99], v[188:191], v[68:71]\n\t"
    "v_max_f32 v254, v208, v212\n\t"
    "v_cmp_lt_f32 vcc, s45, v254\n\t"
    "v_mfma_f32_16x16x32_bf16 v[72:75], a[100:103], v[184:187], v[72:75]\n\t"
    "v_exp_f32 v249, v147\n\t"
    "v_mfma_f32_16x16x32_bf16 v[76:79], a[100:103], v[188:191], v[76:79]\n\t"
    "ds_read_b64_tr_b16 a[148:149], v237 offset:49152\n\t"
    "v_cvt_pk_bf16_f32 v201, v248, v249\n\t"
    "v_mfma_f32_16x16x32_bf16 v[80:83], a[104:107], v[184:187], v[80:83]\n\t"
    "v_exp_f32 v246, v148\n\t"
    "v_mfma_f32_16x16x32_bf16 v[84:87], a[104:107], v[188:191], v[84:87]\n\t"
    "ds_read_b64_tr_b16 a[150:151], v237 offset:53248\n\t"
    "ds_read_b64_tr_b16 a[152:153], v238 offset:49152\n\t"
    "v_mfma_f32_16x16x32_bf16 v[88:91], a[108:111], v[184:187], v[88:91]\n\t"
    "v_exp_f32 v247, v149\n\t"
    "v_mfma_f32_16x16x32_bf16 v[92:95], a[108:111], v[188:191], v[92:95]\n\t"
    "ds_read_b64_tr_b16 a[154:155], v238 offset:53248\n\t"
    "v_cvt_pk_bf16_f32 v202, v246, v247\n\t"
    "v_mfma_f32_16x16x32_bf16 v[96:99], a[112:115], v[184:187], v[96:99]\n\t"
    "v_exp_f32 v248, v150\n\t"
    "v_mfma_f32_16x16x32_bf16 v[100:103], a[112:115], v[188:191], v[100:103]\n\t"
    "s_cbranch_vccnz .Ltsv_stub16\n\t"
    ".Ltsv_back16:\n\t"
    "v_mfma_f32_16x16x32_bf16 v[104:107], a[116:119], v[184:187], v[104:107]\n\t"
    "v_exp_f32 v249, v151\n\t"
    "v_mfma_f32_16x16x32_bf16 v[108:111], a[116:119], v[188:191], v[108:111]\n\t"
    "ds_read_b64_tr_b16 a[156:157], v239 offset:49152\n\t"
    "v_add_u32 v0, s58, v0\n\t"
    "v_cvt_pk_bf16_f32 v203, v248, v249\n\t"
    "v_mfma_f32_16x16x32_bf16 v[112:115], a[120:123], v[184:187], v[112:115]\n\t"
    "v_exp_f32 v246, v152\n\t"
    "v_mfma_f32_16x16x32_bf16 v[216:219], v[224:227], v[200:203], 0\n\t"
    "s_waitcnt lgkmcnt(10)\n\t"
    "ds_read_b64_tr_b16 a[158:159], v239 offset:53248\n\t"
    "v_mfma_f32_16x16x32_bf16 v[116:119], a[120:123], v[188:191], v[116:119]\n\t"
    "v_add_f32 v5, v250, v208\n\t"
    "v_add_f32 v7, v251, v212\n\t"
    "v_mfma_f32_16x16x32_bf16 v[120:123], a[124:127], v[184:187], v[120:123]\n\t"
    "v_exp_f32 v247, v153\n\t"
    "v_mfma_f32_16x16x32_bf16 v[124:127], a[124:127], v[188:191], v[124:127]\n\t"
    "v_mov_b32 v4, v160\n\t"
    "v_cvt_pk_bf16_f32 v204, v246, v247\n\t"
    "s_waitcnt lgkmcnt(0)\n\t"
    "v_mfma_f32_16x16x32_bf16 a[0:3], a[128:131], v[192:195], a[0:3]\n\t"
    "v_exp_f32 v248, v154\n\t"
    "v_mfma_f32_16x16x32_bf16 a[4:7], a[128:131], v[196:199], a[4:7]\n\t"
    "v_exp_f32 v249, v155\n\t"
    "v_mfma_f32_16x16x32_bf16 a[8:11], a[132:135], v[192:195], a[8:11]\n\t"
    "v_cvt_pk_bf16_f32 v205, v248, v249\n\t"
    "v_mov_b32 v6, v164\n\t"
    "v_mfma_f32_16x16x32_bf16 a[12:15], a[132:135], v[196:199], a[12:15]\n\t"
    "ds_write_b128 v2, v[4:7]\n\t"
    "v_exp_f32 v246, v156\n\t"
    "v_mfma_f32_16x16x32_bf16 a[16:19], a[136:139], v[192:195], a[16:19]\n\t"
    "v_exp_f32 v247, v157\n\t"
    "v_mfma_f32_16x16x32_bf16 a[20:23], a[136:139], v[196:199], a[20:23]\n\t"
    "ds_write_b128 v0, a[0:3] offset:0\n\t"
    "v_cvt_pk_bf16_f32 v206, v246, v247\n\t"
    "v_mfma_f32_16x16x32_bf16 a[24:27], a[140:143], v[192:195], a[24:27]\n\t"
    "v_exp_f32 v248, v158\n\t"
    "v_mfma_f32_16x16x32_bf16 a[28:31], a[140:143], v[196:199], a[28:31]\n\t"
    "v_exp_f32 v249, v159\n\t"
    "v_mfma_f32_16x16x32_bf16 a[32:35], a[144:147], v[192:195], a[32:35]\n\t"
    "ds_write_b128 v0, a[4:7] offset:1024\n\t"
    "v_cvt_pk_bf16_f32 v207, v248, v249\n\t"
    "v_mfma_f32_16x16x32_bf16 a[36:39], a[144:147], v[196:199], a[36:39]\n\t"
    "v_xor_b32 v2, 0x400, v2\n\t"
    "v_xor_b32 v1, 0x4000, v0\n\t"
    "v_mfma_f32_16x16x32_bf16 a[40:43], a[148:151], v[192:195], a[40:43]\n\t"
    "v_mfma_f32_16x16x32_bf16 v[220:223], v[224:227], v[204:207], 0\n\t"
    "v_mfma_f32_16x16x32_bf16 a[44:47], a[148:151], v[196:199], a[44:47]\n\t"
    "ds_write_b128 v0, a[8:11] offset:2048\n\t"
    "v_mfma_f32_16x16x32_bf16 a[48:51], a[152:155], v[192:195], a[48:51]\n\t"
    "s_waitcnt lgkmcnt(3)\n\t"
    "s_barrier\n\t"
    "ds_read_b128 v[4:7], v2\n\t"
    "v_mfma_f32_16x16x32_bf16 a[52:55], a[152:155], v[196:199], a[52:55]\n\t"
    "v_mfma_f32_16x16x32_bf16 a[56:59], a[156:159], v[192:195], a[56:59]\n\t"
    "ds_write_b128 v0, a[12:15] offset:3072\n\t"
    "v_max_f32 v254, v216, v220\n\t"
    "v_cmp_lt_f32 vcc, s45, v254\n\t"
    "v_mfma_f32_16x16x32_bf16 a[60:63], a[156:159], v[196:199], a[60:63]\n\t"
    "ds_write_b128 v0, a[16:19] offset:4096\n\t"
    "s_xor_b32 s58, s55, 1\n\t"
    "s_lshl_b32 s58, s58, 13\n\t"
    "s_lshl_b32 s59, s55, 13\n\t"
    "s_add_u32 s59, s59, 65536\n\t"
    "s_cbranch_vccnz .Ltsv_stub17\n\t"
    ".Ltsv_back17:\n\t"
    "v_mfma_f32_16x16x32_bf16 v[64:67], a[128:131], v[200:203], v[64:67]\n\t"
    "v_add_f32 v252, v252, v216\n\t"
    "v_add_f32 v253, v253, v220\n\t"
    "v_mfma_f32_16x16x32_bf16 v[68:71], a[128:131], v[204:207], v[68:71]\n\t"
    "s_waitcnt lgkmcnt(2)\n\t"
    "v_min_f32 v18, v4, v168\n\t"
    "v_min_f32 v19, v6, v172\n\t"
    "v_mfma_f32_16x16x32_bf16 v[72:75], a[132:135], v[200:203], v[72:75]\n\t"
    "ds_write_b128 v0, a[20:23] offset:5120\n\t"
    "v_sub_f32 v10, v18, v168\n\t"
    "v_mfma_f32_16x16x32_bf16 v[76:79], a[132:135], v[204:207], v[76:79]\n\t"
    "v_sub_f32 v12, v18, v4\n\t"
    "v_sub_f32 v14, v19, v172\n\t"
    "v_mfma_f32_16x16x32_bf16 v[80:83], a[136:139], v[200:203], v[80:83]\n\t"
    "v_sub_f32 v16, v19, v6\n\t"
    "v_exp_f32 v10, v10\n\t"
    "v_mfma_f32_16x16x32_bf16 v[84:87], a[136:139], v[204:207], v[84:87]\n\t"
    "ds_write_b128 v0, a[24:27] offset:6144\n\t"
    "v_exp_f32 v12, v12\n\t"
    "v_mfma_f32_16x16x32_bf16 v[88:91], a[140:143], v[200:203], v[88:91]\n\t"
    "v_exp_f32 v14, v14\n\t"
    "v_mfma_f32_16x16x32_bf16 v[92:95], a[140:143], v[204:207], v[92:95]\n\t"
    "v_exp_f32 v16, v16\n\t"
    "v_mfma_f32_16x16x32_bf16 v[96:99], a[144:147], v[200:203], v[96:99]\n\t"
    "ds_write_b128 v0, a[28:31] offset:7168\n\t"
    "v_mul_f32 v18, v10, v252\n\t"
    "v_mfma_f32_16x16x32_bf16 v[100:103], a[144:147], v[204:207], v[100:103]\n\t"
    "v_mul_f32 v19, v14, v253\n\t"
    "v_fma_f32 v18, v12, v5, v18\n\t"
    "v_mfma_f32_16x16x32_bf16 v[104:107], a[148:151], v[200:203], v[104:107]\n\t"
    "v_fma_f32 v19, v16, v7, v19\n\t"
    "v_rcp_f32 v18, v18\n\t"
    "v_mfma_f32_16x16x32_bf16 v[108:111], a[148:151], v[204:207], v[108:111]\n\t"
    "ds_write_b128 v0, a[32:35] offset:8192\n\t"
    "v_rcp_f32 v19, v19\n\t"
    "v_mfma_f32_16x16x32_bf16 v[112:115], a[152:155], v[200:203], v[112:115]\n\t"
    "v_and_b32 v8, 15, v3\n\t"
    "v_mul_f32 v10, v10, v18\n\t"
    "v_mfma_f32_16x16x32_bf16 v[116:119], a[152:155], v[204:207], v[116:119]\n\t"
    "v_mul_f32 v12, v12, v18\n\t"
    "v_mul_f32 v14, v14, v19\n\t"
    "v_mfma_f32_16x16x32_bf16 v[120:123], a[156:159], v[200:203], v[120:123]\n\t"
    "ds_write_b128 v0, a[36:39] offset:9216\n\t"
    "v_mul_f32 v16, v16, v19\n\t"
    "v_mfma_f32_16x16x32_bf16 v[124:127], a[156:159], v[204:207], v[124:127]\n\t"
    "v_lshlrev_b32 v9, 8, v8\n\t"
    "v_and_b32 v8, 7, v3\n\t"
    "ds_write_b128 v0, a[40:43] offset:10240\n\t"
    "v_lshl_add_u32 v9, v8, 4, v9\n\t"
    "v_lshrrev_b32 v8, 4, v3\n\t"
    "v_lshlrev_b32 v20, 3, v8\n\t"
    "v_xor_b32 v20, v20, v9\n\t"
    "v_add_u32 v20, s59, v20\n\t"
    "v_xor_b32 v21, 32, v20\n\t"
    "v_xor_b32 v22, 64, v20\n\t"
    "v_xor_b32 v23, 96, v20\n\t"
    "ds_write_b128 v0, a[44:47] offset:11264\n\t"
    "v_add_u32 v24, s59, v240\n\t"
    "v_add_u32 v25, s59, v241\n\t"
    "v_lshlrev_b32 v26, 4, v3\n\t"
    "v_add_u32 v26, s58, v26\n\t"
    "v_add_u32 v27, 0x1000, v26\n\t"
    "v_pk_mul_f32 v[64:65], v[64:65], v[10:11] op_sel_hi:[1,0]\n\t"
    "v_pk_mul_f32 v[66:67], v[66:67], v[10:11] op_sel_hi:[1,0]\n\t"
    "v_pk_mul_f32 v[72:73], v[72:73], v[10:11] op_sel_hi:[1,0]\n\t"
    "ds_write_b128 v0, a[48:51] offset:12288\n\t"
    "v_pk_mul_f32 v[74:75], v[74:75], v[10:11] op_sel_hi:[1,0]\n\t"
    "v_pk_mul_f32 v[80:81], v[80:81], v[10:11] op_sel_hi:[1,0]\n\t"
    "v_pk_mul_f32 v[82:83], v[82:83], v[10:11] op_sel_hi:[1,0]\n\t"
    "v_pk_mul_f32 v[88:89], v[88:89], v[10:11] op_sel_hi:[1,0]\n\t"
    "v_pk_mul_f32 v[90:91], v[90:91], v[10:11] op_sel_hi:[1,0]\n\t"
    "v_pk_mul_f32 v[96:97], v[96:97], v[10:11] op_sel_hi:[1,0]\n\t"
    "v_pk_mul_f32 v[98:99], v[98:99], v[10:11] op_sel_hi:[1,0]\n\t"
    "v_pk_mul_f32 v[104:105], v[104:105], v[10:11] op_sel_hi:[1,0]\n\t"
    "ds_write_b128 v0, a[52:55] offset:13312\n\t"
    "v_pk_mul_f32 v[106:107], v[106:107], v[10:11] op_sel_hi:[1,0]\n\t"
    "v_pk_mul_f32 v[112:113], v[112:113], v[10:11] op_sel_hi:[1,0]\n\t"
    "v_pk_mul_f32 v[114:115], v[114:115], v[10:11] op_sel_hi:[1,0]\n\t"
    "v_pk_mul_f32 v[120:121], v[120:121], v[10:11] op_sel_hi:[1,0]\n\t"
    "v_pk_mul_f32 v[122:123], v[122:123], v[10:11] op_sel_hi:[1,0]\n\t"
    "v_pk_mul_f32 v[68:69], v[68:69], v[14:15] op_sel_hi:[1,0]\n\t"
    "v_pk_mul_f32 v[70:71], v[70:71], v[14:15] op_sel_hi:[1,0]\n\t"
    "v_pk_mul_f32 v[76:77], v[76:77], v[14:15] op_sel_hi:[1,0]\n\t"
    "ds_write_b128 v0, a[56:59] offset:14336\n\t"
    "v_pk_mul_f32 v[78:79], v[78:79], v[14:15] op_sel_hi:[1,0]\n\t"
    "v_pk_mul_f32 v[84:85], v[84:85], v[14:15] op_sel_hi:[1,0]\n\t"
    "v_pk_mul_f32 v[86:87], v[86:87], v[14:15] op_sel_hi:[1,0]\n\t"
    "v_pk_mul_f32 v[92:93], v[92:93], v[14:15] op_sel_hi:[1,0]\n\t"
    "v_pk_mul_f32 v[94:95], v[94:95], v[14:15] op_sel_hi:[1,0]\n\t"
    "v_pk_mul_f32 v[100:101], v[100:101], v[14:15] op_sel_hi:[1,0]\n\t"
    "v_pk_mul_f32 v[102:103], v[102:103], v[14:15] op_sel_hi:[1,0]\n\t"
    "v_pk_mul_f32 v[108:109], v[108:109], v[14:15] op_sel_hi:[1,0]\n\t"
    "ds_write_b128 v0, a[60:63] offset:15360\n\t"
    "v_pk_mul_f32 v[110:111], v[110:111], v[14:15] op_sel_hi:[1,0]\n\t"
    "v_pk_mul_f32 v[116:117], v[116:117], v[14:15] op_sel_hi:[1,0]\n\t"
    "v_pk_mul_f32 v[118:119], v[118:119], v[14:15] op_sel_hi:[1,0]\n\t"
    "v_pk_mul_f32 v[124:125], v[124:125], v[14:15] op_sel_hi:[1,0]\n\t"
    "v_pk_mul_f32 v[126:127], v[126:127], v[14:15] op_sel_hi:[1,0]\n\t"
    "s_waitcnt lgkmcnt(0)\n\t"
    "s_barrier\n\t"
    "ds_read_b128 v[128:131], v1 offset:0\n\t"
    "ds_read_b128 v[132:135], v1 offset:2048\n\t"
    "ds_read_b128 v[136:139], v1 offset:4096\n\t"
    "ds_read_b128 v[140:143], v1 offset:6144\n\t"
    "ds_read_b128 v[144:147], v1 offset:8192\n\t"
    "ds_read_b128 v[148:151], v1 offset:10240\n\t"
    "ds_read_b128 v[152:155], v1 offset:12288\n\t"
    "ds_read_b128 v[156:159], v1 offset:14336\n\t"
    "ds_read_b128 v[160:163], v1 offset:1024\n\t"
    "ds_read_b128 v[164:167], v1 offset:3072\n\t"
    "ds_read_b128 v[168:171], v1 offset:5120\n\t"
    "ds_read_b128 v[172:175], v1 offset:7168\n\t"
    "ds_read_b128 v[176:179], v1 offset:9216\n\t"
    "ds_read_b128 v[180:183], v1 offset:11264\n\t"
    "s_waitcnt lgkmcnt(12)\n\t"
    "v_pk_fma_f32 v[64:65], v[128:129], v[12:13], v[64:65] op_sel_hi:[1,0,1]\n\t"
    "v_pk_fma_f32 v[66:67], v[130:131], v[12:13], v[66:67] op_sel_hi:[1,0,1]\n\t"
    "v_pk_fma_f32 v[72:73], v[132:133], v[12:13], v[72:73] op_sel_hi:[1,0,1]\n\t"
    "v_pk_fma_f32 v[74:75], v[134:135], v[12:13], v[74:75] op_sel_hi:[1,0,1]\n\t"
    "v_cvt_pk_bf16_f32 v32, v64, v65\n\t"
    "v_cvt_pk_bf16_f32 v33, v66, v67\n\t"
    "v_cvt_pk_bf16_f32 v34, v72, v73\n\t"
    "v_cvt_pk_bf16_f32 v35, v74, v75\n\t"
    "ds_write_b64 v20, v[32:33] offset:0\n\t"
    "ds_write_b64 v21, v[34:35] offset:0\n\t"
    "s_waitcnt lgkmcnt(12)\n\t"
    "ds_read_b128 v[184:187], v1 offset:13312\n\t"
    "ds_read_b128 v[188:191], v1 offset:15360\n\t"
    "v_pk_fma_f32 v[80:81], v[136:137], v[12:13], v[80:81] op_sel_hi:[1,0,1]\n\t"
    "v_pk_fma_f32 v[82:83], v[138:139], v[12:13], v[82:83] op_sel_hi:[1,0,1]\n\t"
    "v_pk_fma_f32 v[88:89], v[140:141], v[12:13], v[88:89] op_sel_hi:[1,0,1]\n\t"
    "v_pk_fma_f32 v[90:91], v[142:143], v[12:13], v[90:91] op_sel_hi:[1,0,1]\n\t"
    "v_cvt_pk_bf16_f32 v36, v80, v81\n\t"
    "v_cvt_pk_bf16_f32 v37, v82, v83\n\t"
    "v_cvt_pk_bf16_f32 v38, v88, v89\n\t"
    "v_cvt_pk_bf16_f32 v39, v90, v91\n\t"
    "ds_write_b64 v22, v[36:37] offset:0\n\t"
    "s_waitcnt lgkmcnt(10)\n\t"
    "ds_write_b64 v23, v[38:39] offset:0\n\t"
    "s_waitcnt lgkmcnt(11)\n\t"
    "v_pk_fma_f32 v[96:97], v[144:145], v[12:13], v[96:97] op_sel_hi:[1,0,1]\n\t"
    "v_pk_fma_f32 v[98:99], v[146:147], v[12:13], v[98:99] op_sel_hi:[1,0,1]\n\t"
    "v_pk_fma_f32 v[104:105], v[148:149], v[12:13], v[104:105] op_sel_hi:[1,0,1]\n\t"
    "v_pk_fma_f32 v[106:107], v[150:151], v[12:13], v[106:107] op_sel_hi:[1,0,1]\n\t"
    "v_cvt_pk_bf16_f32 v40, v96, v97\n\t"
    "v_cvt_pk_bf16_f32 v41, v98, v99\n\t"
    "v_cvt_pk_bf16_f32 v42, v104, v105\n\t"
    "v_cvt_pk_bf16_f32 v43, v106, v107\n\t"
    "ds_write_b64 v20, v[40:41] offset:128\n\t"
    "ds_write_b64 v21, v[42:43] offset:128\n\t"
    "s_waitcnt lgkmcnt(13)\n\t"
    "v_pk_fma_f32 v[112:113], v[152:153], v[12:13], v[112:113] op_sel_hi:[1,0,1]\n\t"
    "v_pk_fma_f32 v[114:115], v[154:155], v[12:13], v[114:115] op_sel_hi:[1,0,1]\n\t"
    "v_pk_fma_f32 v[120:121], v[156:157], v[12:13], v[120:121] op_sel_hi:[1,0,1]\n\t"
    "v_pk_fma_f32 v[122:123], v[158:159], v[12:13], v[122:123] op_sel_hi:[1,0,1]\n\t"
    "v_cvt_pk_bf16_f32 v44, v112, v113\n\t"
    "v_cvt_pk_bf16_f32 v45, v114, v115\n\t"
    "v_cvt_pk_bf16_f32 v46, v120, v121\n\t"
    "v_cvt_pk_bf16_f32 v47, v122, v123\n\t"
    "ds_write_b64 v22, v[44:45] offset:128\n\t"
    "ds_write_b64 v23, v[46:47] offset:128\n\t"
    "s_waitcnt lgkmcnt(10)\n\t"
    "ds_read_b128 v[192:195], v24 offset:0\n\t"
    "ds_read_b128 v[196:199], v25 offset:1024\n\t"
    "ds_read_b128 v[200:203], v24 offset:2048\n\t"
    "ds_read_b128 v[204:207], v25 offset:3072\n\t"
    "s_waitcnt lgkmcnt(14)\n\t"
    "v_pk_fma_f32 v[68:69], v[160:161], v[16:17], v[68:69] op_sel_hi:[1,0,1]\n\t"
    "v_pk_fma_f32 v[70:71], v[162:163], v[16:17], v[70:71] op_sel_hi:[1,0,1]\n\t"
    "v_pk_fma_f32 v[76:77], v[164:165], v[16:17], v[76:77] op_sel_hi:[1,0,1]\n\t"
    "v_pk_fma_f32 v[78:79], v[166:167], v[16:17], v[78:79] op_sel_hi:[1,0,1]\n\t"
    "v_cvt_pk_bf16_f32 v48, v68, v69\n\t"
    "v_cvt_pk_bf16_f32 v49, v70, v71\n\t"
    "v_cvt_pk_bf16_f32 v50, v76, v77\n\t"
    "v_cvt_pk_bf16_f32 v51, v78, v79\n\t"
    "ds_write_b64 v20, v[48:49] offset:4096\n\t"
    "s_waitcnt lgkmcnt(10)\n\t"
    "ds_write_b64 v21, v[50:51] offset:4096\n\t"
    "s_waitcnt lgkmcnt(11)\n\t"
    "v_pk_fma_f32 v[84:85], v[168:169], v[16:17], v[84:85] op_sel_hi:[1,0,1]\n\t"
    "v_pk_fma_f32 v[86:87], v[170:171], v[16:17], v[86:87] op_sel_hi:[1,0,1]\n\t"
    "v_pk_fma_f32 v[92:93], v[172:173], v[16:17], v[92:93] op_sel_hi:[1,0,1]\n\t"
    "v_pk_fma_f32 v[94:95], v[174:175], v[16:17], v[94:95] op_sel_hi:[1,0,1]\n\t"
    "v_cvt_pk_bf16_f32 v52, v84, v85\n\t"
    "v_cvt_pk_bf16_f32 v53, v86, v87\n\t"
    "v_cvt_pk_bf16_f32 v54, v92, v93\n\t"
    "v_cvt_pk_bf16_f32 v55, v94, v95\n\t"
    "ds_write_b64 v22, v[52:53] offset:4096\n\t"
    "ds_write_b64 v23, v[54:55] offset:4096\n\t"
    "s_waitcnt lgkmcnt(7)\n\t"
    "global_store_dwordx4 v26, v[192:195], s[20:21] offset:0 sc0 sc1 nt\n\t"
    "s_waitcnt lgkmcnt(6)\n\t"
    "global_store_dwordx4 v26, v[196:199], s[20:21] offset:1024 sc0 sc1 nt\n\t"
    "s_waitcnt lgkmcnt(5)\n\t"
    "global_store_dwordx4 v26, v[200:203], s[20:21] offset:2048 sc0 sc1 nt\n\t"
    "s_waitcnt lgkmcnt(4)\n\t"
    "global_store_dwordx4 v26, v[204:207], s[20:21] offset:3072 sc0 sc1 nt\n\t"
    "s_waitcnt lgkmcnt(4)\n\t"
    "v_pk_fma_f32 v[100:101], v[176:177], v[16:17], v[100:101] op_sel_hi:[1,0,1]\n\t"
    "v_pk_fma_f32 v[102:103], v[178:179], v[16:17], v[102:103] op_sel_hi:[1,0,1]\n\t"
    "v_pk_fma_f32 v[108:109], v[180:181], v[16:17], v[108:109] op_sel_hi:[1,0,1]\n\t"
    "v_pk_fma_f32 v[110:111], v[182:183], v[16:17], v[110:111] op_sel_hi:[1,0,1]\n\t"
    "v_cvt_pk_bf16_f32 v56, v100, v101\n\t"
    "v_cvt_pk_bf16_f32 v57, v102, v103\n\t"
    "v_cvt_pk_bf16_f32 v58, v108, v109\n\t"
    "v_cvt_pk_bf16_f32 v59, v110, v111\n\t"
    "ds_write_b64 v20, v[56:57] offset:4224\n\t"
    "ds_write_b64 v21, v[58:59] offset:4224\n\t"
    "s_waitcnt lgkmcnt(6)\n\t"
    "v_pk_fma_f32 v[116:117], v[184:185], v[16:17], v[116:117] op_sel_hi:[1,0,1]\n\t"
    "v_pk_fma_f32 v[118:119], v[186:187], v[16:17], v[118:119] op_sel_hi:[1,0,1]\n\t"
    "v_pk_fma_f32 v[124:125], v[188:189], v[16:17], v[124:125] op_sel_hi:[1,0,1]\n\t"
    "v_pk_fma_f32 v[126:127], v[190:191], v[16:17], v[126:127] op_sel_hi:[1,0,1]\n\t"
    "v_cvt_pk_bf16_f32 v60, v116, v117\n\t"
    "v_cvt_pk_bf16_f32 v61, v118, v119\n\t"
    "v_cvt_pk_bf16_f32 v62, v124, v125\n\t"
    "v_cvt_pk_bf16_f32 v63, v126, v127\n\t"
    "ds_write_b64 v22, v[60:61] offset:4224\n\t"
    "ds_write_b64 v23, v[62:63] offset:4224\n\t"
    "ds_read_b128 v[208:211], v24 offset:4096\n\t"
    "ds_read_b128 v[212:215], v25 offset:5120\n\t"
    "ds_read_b128 v[216:219], v24 offset:6144\n\t"
    "ds_read_b128 v[220:223], v25 offset:7168\n\t"
    "s_waitcnt lgkmcnt(3)\n\t"
    "global_store_dwordx4 v27, v[208:211], s[20:21] offset:0 sc0 sc1 nt\n\t"
    "s_waitcnt lgkmcnt(2)\n\t"
    "global_store_dwordx4 v27, v[212:215], s[20:21] offset:1024 sc0 sc1 nt\n\t"
    "s_waitcnt lgkmcnt(1)\n\t"
    "global_store_dwordx4 v27, v[216:219], s[20:21] offset:2048 sc0 sc1 nt\n\t"
    "s_waitcnt lgkmcnt(0)\n\t"
    "global_store_dwordx4 v27, v[220:223], s[20:21] offset:3072 sc0 sc1 nt\n\t"
    "s_endpgm\n\t"
    ".Ltsv_mainend:\n\t"
    ".Ltsv_stub0:\n\t"
    "s_getpc_b64 s[50:51]\n\t"
    ".Ltsv_sa0:\n\t"
    "s_add_u32 s50, s50, .Ltsv_rare01-.Ltsv_sa0\n\t"
    "s_addc_u32 s51, s51, 0\n\t"
    "s_swappc_b64 s[52:53], s[50:51]\n\t"
    "s_branch .Ltsv_back0\n\t"
    ".Ltsv_stub1:\n\t"
    "s_getpc_b64 s[50:51]\n\t"
    ".Ltsv_sa1:\n\t"
    "s_add_u32 s50, s50, .Ltsv_rare11-.Ltsv_sa1\n\t"
    "s_addc_u32 s51, s51, 0\n\t"
    "s_swappc_b64 s[52:53], s[50:51]\n\t"
    "s_branch .Ltsv_back1\n\t"
    ".Ltsv_stub3:\n\t"
    "s_getpc_b64 s[50:51]\n\t"
    ".Ltsv_sa3:\n\t"
    "s_add_u32 s50, s50, .Ltsv_rare00-.Ltsv_sa3\n\t"
    "s_addc_u32 s51, s51, 0\n\t"
    "s_swappc_b64 s[52:53], s[50:51]\n\t"
    "s_branch .Ltsv_back3\n\t"
    ".Ltsv_stub4:\n\t"
    "s_getpc_b64 s[50:51]\n\t"
    ".Ltsv_sa4:\n\t"
    "s_add_u32 s50, s50, .Ltsv_rare10-.Ltsv_sa4\n\t"
    "s_addc_u32 s51, s51, 0\n\t"
    "s_swappc_b64 s[52:53], s[50:51]\n\t"
    "s_branch .Ltsv_back4\n\t"
    ".Ltsv_stub6:\n\t"
    "s_getpc_b64 s[50:51]\n\t"
    ".Ltsv_sa6:\n\t"
    "s_add_u32 s50, s50, .Ltsv_rare01-.Ltsv_sa6\n\t"
    "s_addc_u32 s51, s51, 0\n\t"
    "s_swappc_b64 s[52:53], s[50:51]\n\t"
    "s_branch .Ltsv_back6\n\t"
    ".Ltsv_stub7:\n\t"
    "s_getpc_b64 s[50:51]\n\t"
    ".Ltsv_sa7:\n\t"
    "s_add_u32 s50, s50, .Ltsv_rare11-.Ltsv_sa7\n\t"
    "s_addc_u32 s51, s51, 0\n\t"
    "s_swappc_b64 s[52:53], s[50:51]\n\t"
    "s_branch .Ltsv_back7\n\t"
    ".Ltsv_stub9:\n\t"
    "s_getpc_b64 s[50:51]\n\t"
    ".Ltsv_sa9:\n\t"
    "s_add_u32 s50, s50, .Ltsv_rare00-.Ltsv_sa9\n\t"
    "s_addc_u32 s51, s51, 0\n\t"
    "s_swappc_b64 s[52:53], s[50:51]\n\t"
    "s_branch .Ltsv_back9\n\t"
    ".Ltsv_stub10:\n\t"
    "s_getpc_b64 s[50:51]\n\t"
    ".Ltsv_sa10:\n\t"
    "s_add_u32 s50, s50, .Ltsv_rare10-.Ltsv_sa10\n\t"
    "s_addc_u32 s51, s51, 0\n\t"
    "s_swappc_b64 s[52:53], s[50:51]\n\t"
    "s_branch .Ltsv_back10\n\t"
    ".Ltsv_stub12:\n\t"
    "s_getpc_b64 s[50:51]\n\t"
    ".Ltsv_sa12:\n\t"
    "s_add_u32 s50, s50, .Ltsv_rare01-.Ltsv_sa12\n\t"
    "s_addc_u32 s51, s51, 0\n\t"
    "s_swappc_b64 s[52:53], s[50:51]\n\t"
    "s_branch .Ltsv_back12\n\t"
    ".Ltsv_stub13:\n\t"
    "s_getpc_b64 s[50:51]\n\t"
    ".Ltsv_sa13:\n\t"
    "s_add_u32 s50, s50, .Ltsv_rare11-.Ltsv_sa13\n\t"
    "s_addc_u32 s51, s51, 0\n\t"
    "s_swappc_b64 s[52:53], s[50:51]\n\t"
    "s_branch .Ltsv_back13\n\t"
    ".Ltsv_stub14:\n\t"
    "s_getpc_b64 s[50:51]\n\t"
    ".Ltsv_sa14:\n\t"
    "s_add_u32 s50, s50, .Ltsv_rare00-.Ltsv_sa14\n\t"
    "s_addc_u32 s51, s51, 0\n\t"
    "s_swappc_b64 s[52:53], s[50:51]\n\t"
    "s_branch .Ltsv_back14\n\t"
    ".Ltsv_stub15:\n\t"
    "s_getpc_b64 s[50:51]\n\t"
    ".Ltsv_sa15:\n\t"
    "s_add_u32 s50, s50, .Ltsv_rare10-.Ltsv_sa15\n\t"
    "s_addc_u32 s51, s51, 0\n\t"
    "s_swappc_b64 s[52:53], s[50:51]\n\t"
    "s_branch .Ltsv_back15\n\t"
    ".Ltsv_stub16:\n\t"
    "s_getpc_b64 s[50:51]\n\t"
    ".Ltsv_sa16:\n\t"
    "s_add_u32 s50, s50, .Ltsv_rare01-.Ltsv_sa16\n\t"
    "s_addc_u32 s51, s51, 0\n\t"
    "s_swappc_b64 s[52:53], s[50:51]\n\t"
    "s_branch .Ltsv_back16\n\t"
    ".Ltsv_stub17:\n\t"
    "s_getpc_b64 s[50:51]\n\t"
    ".Ltsv_sa17:\n\t"
    "s_add_u32 s50, s50, .Ltsv_rare11-.Ltsv_sa17\n\t"
    "s_addc_u32 s51, s51, 0\n\t"
    "s_swappc_b64 s[52:53], s[50:51]\n\t"
    "s_branch .Ltsv_back17\n\t"
    ".Ltsv_rare00:\n\t"
    "s_nop 15\n\t"
    "v_max3_f32 v209, v128, v129, v130\n\t"
    "v_max3_f32 v209, v209, v131, v132\n\t"
    "v_max3_f32 v209, v209, v133, v134\n\t"
    "v_max_f32 v209, v209, v135\n\t"
    "v_max3_f32 v213, v136, v137, v138\n\t"
    "v_max3_f32 v213, v213, v139, v140\n\t"
    "v_max3_f32 v213, v213, v141, v142\n\t"
    "v_max_f32 v213, v213, v143\n\t"
    "s_nop 1\n\t"
    "v_permlane32_swap_b32 v209, v213\n\t"
    "v_max_f32 v209, v209, v213\n\t"
    "v_mov_b32 v213, v209\n\t"
    "s_nop 1\n\t"
    "v_permlane16_swap_b32 v209, v213\n\t"
    "v_max_f32 v209, v209, v213\n\t"
    "v_mov_b32 v213, v209\n\t"
    "s_nop 1\n\t"
    "v_permlane32_swap_b32 v209, v213\n\t"
    "v_max_f32 v209, 0, v209\n\t"
    "v_max_f32 v213, 0, v213\n\t"
    "v_exp_f32 v210, -v209\n\t"
    "v_exp_f32 v214, -v213\n\t"
    "v_sub_f32 v160, v160, v209\n\t"
    "v_sub_f32 v161, v161, v209\n\t"
    "v_sub_f32 v162, v162, v209\n\t"
    "v_sub_f32 v163, v163, v209\n\t"
    "v_mul_f32 v250, v250, v210\n\t"
    "v_sub_f32 v164, v164, v213\n\t"
    "v_sub_f32 v165, v165, v213\n\t"
    "v_sub_f32 v166, v166, v213\n\t"
    "v_sub_f32 v167, v167, v213\n\t"
    "v_mul_f32 v251, v251, v214\n\t"
    "v_accvgpr_read_b32 v211, a0\n\t"
    "v_mul_f32 v211, v211, v210\n\t"
    "v_accvgpr_write_b32 a0, v211\n\t"
    "v_accvgpr_read_b32 v211, a1\n\t"
    "v_mul_f32 v211, v211, v210\n\t"
    "v_accvgpr_write_b32 a1, v211\n\t"
    "v_accvgpr_read_b32 v211, a2\n\t"
    "v_mul_f32 v211, v211, v210\n\t"
    "v_accvgpr_write_b32 a2, v211\n\t"
    "v_accvgpr_read_b32 v211, a3\n\t"
    "v_mul_f32 v211, v211, v210\n\t"
    "v_accvgpr_write_b32 a3, v211\n\t"
    "v_accvgpr_read_b32 v211, a4\n\t"
    "v_mul_f32 v211, v211, v214\n\t"
    "v_accvgpr_write_b32 a4, v211\n\t"
    "v_accvgpr_read_b32 v211, a5\n\t"
    "v_mul_f32 v211, v211, v214\n\t"
    "v_accvgpr_write_b32 a5, v211\n\t"
    "v_accvgpr_read_b32 v211, a6\n\t"
    "v_mul_f32 v211, v211, v214\n\t"
    "v_accvgpr_write_b32 a6, v211\n\t"
    "v_accvgpr_read_b32 v211, a7\n\t"
    "v_mul_f32 v211, v211, v214\n\t"
    "v_accvgpr_write_b32 a7, v211\n\t"
    "v_accvgpr_read_b32 v211, a8\n\t"
    "v_mul_f32 v211, v211, v210\n\t"
    "v_accvgpr_write_b32 a8, v211\n\t"
    "v_accvgpr_read_b32 v211, a9\n\t"
    "v_mul_f32 v211, v211, v210\n\t"
    "v_accvgpr_write_b32 a9, v211\n\t"
    "v_accvgpr_read_b32 v211, a10\n\t"
    "v_mul_f32 v211, v211, v210\n\t"
    "v_accvgpr_write_b32 a10, v211\n\t"
    "v_accvgpr_read_b32 v211, a11\n\t"
    "v_mul_f32 v211, v211, v210\n\t"
    "v_accvgpr_write_b32 a11, v211\n\t"
    "v_accvgpr_read_b32 v211, a12\n\t"
    "v_mul_f32 v211, v211, v214\n\t"
    "v_accvgpr_write_b32 a12, v211\n\t"
    "v_accvgpr_read_b32 v211, a13\n\t"
    "v_mul_f32 v211, v211, v214\n\t"
    "v_accvgpr_write_b32 a13, v211\n\t"
    "v_accvgpr_read_b32 v211, a14\n\t"
    "v_mul_f32 v211, v211, v214\n\t"
    "v_accvgpr_write_b32 a14, v211\n\t"
    "v_accvgpr_read_b32 v211, a15\n\t"
    "v_mul_f32 v211, v211, v214\n\t"
    "v_accvgpr_write_b32 a15, v211\n\t"
    "v_accvgpr_read_b32 v211, a16\n\t"
    "v_mul_f32 v211, v211, v210\n\t"
    "v_accvgpr_write_b32 a16, v211\n\t"
    "v_accvgpr_read_b32 v211, a17\n\t"
    "v_mul_f32 v211, v211, v210\n\t"
    "v_accvgpr_write_b32 a17, v211\n\t"
    "v_accvgpr_read_b32 v211, a18\n\t"
    "v_mul_f32 v211, v211, v210\n\t"
    "v_accvgpr_write_b32 a18, v211\n\t"
    "v_accvgpr_read_b32 v211, a19\n\t"
    "v_mul_f32 v211, v211, v210\n\t"
    "v_accvgpr_write_b32 a19, v211\n\t"
    "v_accvgpr_read_b32 v211, a20\n\t"
    "v_mul_f32 v211, v211, v214\n\t"
    "v_accvgpr_write_b32 a20, v211\n\t"
    "v_accvgpr_read_b32 v211, a21\n\t"
    "v_mul_f32 v211, v211, v214\n\t"
    "v_accvgpr_write_b32 a21, v211\n\t"
    "v_accvgpr_read_b32 v211, a22\n\t"
    "v_mul_f32 v211, v211, v214\n\t"
    "v_accvgpr_write_b32 a22, v211\n\t"
    "v_accvgpr_read_b32 v211, a23\n\t"
    "v_mul_f32 v211, v211, v214\n\t"
    "v_accvgpr_write_b32 a23, v211\n\t"
    "v_accvgpr_read_b32 v211, a24\n\t"
    "v_mul_f32 v211, v211, v210\n\t"
    "v_accvgpr_write_b32 a24, v211\n\t"
    "v_accvgpr_read_b32 v211, a25\n\t"
    "v_mul_f32 v211, v211, v210\n\t"
    "v_accvgpr_write_b32 a25, v211\n\t"
    "v_accvgpr_read_b32 v211, a26\n\t"
    "v_mul_f32 v211, v211, v210\n\t"
    "v_accvgpr_write_b32 a26, v211\n\t"
    "v_accvgpr_read_b32 v211, a27\n\t"
    "v_mul_f32 v211, v211, v210\n\t"
    "v_accvgpr_write_b32 a27, v211\n\t"
    "v_accvgpr_read_b32 v211, a28\n\t"
    "v_mul_f32 v211, v211, v214\n\t"
    "v_accvgpr_write_b32 a28, v211\n\t"
    "v_accvgpr_read_b32 v211, a29\n\t"
    "v_mul_f32 v211, v211, v214\n\t"
    "v_accvgpr_write_b32 a29, v211\n\t"
    "v_accvgpr_read_b32 v211, a30\n\t"
    "v_mul_f32 v211, v211, v214\n\t"
    "v_accvgpr_write_b32 a30, v211\n\t"
    "v_accvgpr_read_b32 v211, a31\n\t"
    "v_mul_f32 v211, v211, v214\n\t"
    "v_accvgpr_write_b32 a31, v211\n\t"
    "v_accvgpr_read_b32 v211, a32\n\t"
    "v_mul_f32 v211, v211, v210\n\t"
    "v_accvgpr_write_b32 a32, v211\n\t"
    "v_accvgpr_read_b32 v211, a33\n\t"
    "v_mul_f32 v211, v211, v210\n\t"
    "v_accvgpr_write_b32 a33, v211\n\t"
    "v_accvgpr_read_b32 v211, a34\n\t"
    "v_mul_f32 v211, v211, v210\n\t"
    "v_accvgpr_write_b32 a34, v211\n\t"
    "v_accvgpr_read_b32 v211, a35\n\t"
    "v_mul_f32 v211, v211, v210\n\t"
    "v_accvgpr_write_b32 a35, v211\n\t"
    "v_accvgpr_read_b32 v211, a36\n\t"
    "v_mul_f32 v211, v211, v214\n\t"
    "v_accvgpr_write_b32 a36, v211\n\t"
    "v_accvgpr_read_b32 v211, a37\n\t"
    "v_mul_f32 v211, v211, v214\n\t"
    "v_accvgpr_write_b32 a37, v211\n\t"
    "v_accvgpr_read_b32 v211, a38\n\t"
    "v_mul_f32 v211, v211, v214\n\t"
    "v_accvgpr_write_b32 a38, v211\n\t"
    "v_accvgpr_read_b32 v211, a39\n\t"
    "v_mul_f32 v211, v211, v214\n\t"
    "v_accvgpr_write_b32 a39, v211\n\t"
    "v_accvgpr_read_b32 v211, a40\n\t"
    "v_mul_f32 v211, v211, v210\n\t"
    "v_accvgpr_write_b32 a40, v211\n\t"
    "v_accvgpr_read_b32 v211, a41\n\t"
    "v_mul_f32 v211, v211, v210\n\t"
    "v_accvgpr_write_b32 a41, v211\n\t"
    "v_accvgpr_read_b32 v211, a42\n\t"
    "v_mul_f32 v211, v211, v210\n\t"
    "v_accvgpr_write_b32 a42, v211\n\t"
    "v_accvgpr_read_b32 v211, a43\n\t"
    "v_mul_f32 v211, v211, v210\n\t"
    "v_accvgpr_write_b32 a43, v211\n\t"
    "v_accvgpr_read_b32 v211, a44\n\t"
    "v_mul_f32 v211, v211, v214\n\t"
    "v_accvgpr_write_b32 a44, v211\n\t"
    "v_accvgpr_read_b32 v211, a45\n\t"
    "v_mul_f32 v211, v211, v214\n\t"
    "v_accvgpr_write_b32 a45, v211\n\t"
    "v_accvgpr_read_b32 v211, a46\n\t"
    "v_mul_f32 v211, v211, v214\n\t"
    "v_accvgpr_write_b32 a46, v211\n\t"
    "v_accvgpr_read_b32 v211, a47\n\t"
    "v_mul_f32 v211, v211, v214\n\t"
    "v_accvgpr_write_b32 a47, v211\n\t"
    "v_accvgpr_read_b32 v211, a48\n\t"
    "v_mul_f32 v211, v211, v210\n\t"
    "v_accvgpr_write_b32 a48, v211\n\t"
    "v_accvgpr_read_b32 v211, a49\n\t"
    "v_mul_f32 v211, v211, v210\n\t"
    "v_accvgpr_write_b32 a49, v211\n\t"
    "v_accvgpr_read_b32 v211, a50\n\t"
    "v_mul_f32 v211, v211, v210\n\t"
    "v_accvgpr_write_b32 a50, v211\n\t"
    "v_accvgpr_read_b32 v211, a51\n\t"
    "v_mul_f32 v211, v211, v210\n\t"
    "v_accvgpr_write_b32 a51, v211\n\t"
    "v_accvgpr_read_b32 v211, a52\n\t"
    "v_mul_f32 v211, v211, v214\n\t"
    "v_accvgpr_write_b32 a52, v211\n\t"
    "v_accvgpr_read_b32 v211, a53\n\t"
    "v_mul_f32 v211, v211, v214\n\t"
    "v_accvgpr_write_b32 a53, v211\n\t"
    "v_accvgpr_read_b32 v211, a54\n\t"
    "v_mul_f32 v211, v211, v214\n\t"
    "v_accvgpr_write_b32 a54, v211\n\t"
    "v_accvgpr_read_b32 v211, a55\n\t"
    "v_mul_f32 v211, v211, v214\n\t"
    "v_accvgpr_write_b32 a55, v211\n\t"
    "v_accvgpr_read_b32 v211, a56\n\t"
    "v_mul_f32 v211, v211, v210\n\t"
    "v_accvgpr_write_b32 a56, v211\n\t"
    "v_accvgpr_read_b32 v211, a57\n\t"
    "v_mul_f32 v211, v211, v210\n\t"
    "v_accvgpr_write_b32 a57, v211\n\t"
    "v_accvgpr_read_b32 v211, a58\n\t"
    "v_mul_f32 v211, v211, v210\n\t"
    "v_accvgpr_write_b32 a58, v211\n\t"
    "v_accvgpr_read_b32 v211, a59\n\t"
    "v_mul_f32 v211, v211, v210\n\t"
    "v_accvgpr_write_b32 a59, v211\n\t"
    "v_accvgpr_read_b32 v211, a60\n\t"
    "v_mul_f32 v211, v211, v214\n\t"
    "v_accvgpr_write_b32 a60, v211\n\t"
    "v_accvgpr_read_b32 v211, a61\n\t"
    "v_mul_f32 v211, v211, v214\n\t"
    "v_accvgpr_write_b32 a61, v211\n\t"
    "v_accvgpr_read_b32 v211, a62\n\t"
    "v_mul_f32 v211, v211, v214\n\t"
    "v_accvgpr_write_b32 a62, v211\n\t"
    "v_accvgpr_read_b32 v211, a63\n\t"
    "v_mul_f32 v211, v211, v214\n\t"
    "v_accvgpr_write_b32 a63, v211\n\t"
    "v_sub_f32 v128, v128, v209\n\t"
    "v_sub_f32 v129, v129, v209\n\t"
    "v_sub_f32 v130, v130, v209\n\t"
    "v_sub_f32 v131, v131, v209\n\t"
    "v_sub_f32 v132, v132, v209\n\t"
    "v_sub_f32 v133, v133, v209\n\t"
    "v_sub_f32 v134, v134, v209\n\t"
    "v_sub_f32 v135, v135, v209\n\t"
    "v_sub_f32 v136, v136, v213\n\t"
    "v_sub_f32 v137, v137, v213\n\t"
    "v_sub_f32 v138, v138, v213\n\t"
    "v_sub_f32 v139, v139, v213\n\t"
    "v_sub_f32 v140, v140, v213\n\t"
    "v_sub_f32 v141, v141, v213\n\t"
    "v_sub_f32 v142, v142, v213\n\t"
    "v_sub_f32 v143, v143, v213\n\t"
    "v_exp_f32 v128, v128\n\t"
    "v_exp_f32 v129, v129\n\t"
    "v_exp_f32 v130, v130\n\t"
    "v_exp_f32 v131, v131\n\t"
    "v_exp_f32 v132, v132\n\t"
    "v_exp_f32 v133, v133\n\t"
    "v_exp_f32 v134, v134\n\t"
    "v_exp_f32 v135, v135\n\t"
    "v_exp_f32 v136, v136\n\t"
    "v_exp_f32 v137, v137\n\t"
    "v_exp_f32 v138, v138\n\t"
    "v_exp_f32 v139, v139\n\t"
    "v_exp_f32 v140, v140\n\t"
    "v_exp_f32 v141, v141\n\t"
    "v_exp_f32 v142, v142\n\t"
    "v_exp_f32 v143, v143\n\t"
    "s_nop 0\n\t"
    "v_cvt_pk_bf16_f32 v176, v128, v129\n\t"
    "v_cvt_pk_bf16_f32 v177, v130, v131\n\t"
    "v_cvt_pk_bf16_f32 v178, v132, v133\n\t"
    "v_cvt_pk_bf16_f32 v179, v134, v135\n\t"
    "v_cvt_pk_bf16_f32 v180, v136, v137\n\t"
    "v_cvt_pk_bf16_f32 v181, v138, v139\n\t"
    "v_cvt_pk_bf16_f32 v182, v140, v141\n\t"
    "v_cvt_pk_bf16_f32 v183, v142, v143\n\t"
    "s_nop 1\n\t"
    "v_mfma_f32_16x16x32_bf16 v[208:211], v[224:227], v[176:179], 0\n\t"
    "v_mfma_f32_16x16x32_bf16 v[212:215], v[224:227], v[180:183], 0\n\t"
    "s_nop 15\n\t"
    "s_setpc_b64 s[52:53]\n\t"
    ".Ltsv_rare01:\n\t"
    "s_nop 15\n\t"
    "v_max3_f32 v209, v128, v129, v130\n\t"
    "v_max3_f32 v209, v209, v131, v132\n\t"
    "v_max3_f32 v209, v209, v133, v134\n\t"
    "v_max_f32 v209, v209, v135\n\t"
    "v_max3_f32 v213, v136, v137, v138\n\t"
    "v_max3_f32 v213, v213, v139, v140\n\t"
    "v_max3_f32 v213, v213, v141, v142\n\t"
    "v_max_f32 v213, v213, v143\n\t"
    "s_nop 1\n\t"
    "v_permlane32_swap_b32 v209, v213\n\t"
    "v_max_f32 v209, v209, v213\n\t"
    "v_mov_b32 v213, v209\n\t"
    "s_nop 1\n\t"
    "v_permlane16_swap_b32 v209, v213\n\t"
    "v_max_f32 v209, v209, v213\n\t"
    "v_mov_b32 v213, v209\n\t"
    "s_nop 1\n\t"
    "v_permlane32_swap_b32 v209, v213\n\t"
    "v_max_f32 v209, 0, v209\n\t"
    "v_max_f32 v213, 0, v213\n\t"
    "v_exp_f32 v210, -v209\n\t"
    "v_exp_f32 v214, -v213\n\t"
    "v_sub_f32 v160, v160, v209\n\t"
    "v_sub_f32 v161, v161, v209\n\t"
    "v_sub_f32 v162, v162, v209\n\t"
    "v_sub_f32 v163, v163, v209\n\t"
    "v_mul_f32 v250, v250, v210\n\t"
    "v_sub_f32 v164, v164, v213\n\t"
    "v_sub_f32 v165, v165, v213\n\t"
    "v_sub_f32 v166, v166, v213\n\t"
    "v_sub_f32 v167, v167, v213\n\t"
    "v_mul_f32 v251, v251, v214\n\t"
    "v_accvgpr_read_b32 v211, a0\n\t"
    "v_mul_f32 v211, v211, v210\n\t"
    "v_accvgpr_write_b32 a0, v211\n\t"
    "v_accvgpr_read_b32 v211, a1\n\t"
    "v_mul_f32 v211, v211, v210\n\t"
    "v_accvgpr_write_b32 a1, v211\n\t"
    "v_accvgpr_read_b32 v211, a2\n\t"
    "v_mul_f32 v211, v211, v210\n\t"
    "v_accvgpr_write_b32 a2, v211\n\t"
    "v_accvgpr_read_b32 v211, a3\n\t"
    "v_mul_f32 v211, v211, v210\n\t"
    "v_accvgpr_write_b32 a3, v211\n\t"
    "v_accvgpr_read_b32 v211, a4\n\t"
    "v_mul_f32 v211, v211, v214\n\t"
    "v_accvgpr_write_b32 a4, v211\n\t"
    "v_accvgpr_read_b32 v211, a5\n\t"
    "v_mul_f32 v211, v211, v214\n\t"
    "v_accvgpr_write_b32 a5, v211\n\t"
    "v_accvgpr_read_b32 v211, a6\n\t"
    "v_mul_f32 v211, v211, v214\n\t"
    "v_accvgpr_write_b32 a6, v211\n\t"
    "v_accvgpr_read_b32 v211, a7\n\t"
    "v_mul_f32 v211, v211, v214\n\t"
    "v_accvgpr_write_b32 a7, v211\n\t"
    "v_accvgpr_read_b32 v211, a8\n\t"
    "v_mul_f32 v211, v211, v210\n\t"
    "v_accvgpr_write_b32 a8, v211\n\t"
    "v_accvgpr_read_b32 v211, a9\n\t"
    "v_mul_f32 v211, v211, v210\n\t"
    "v_accvgpr_write_b32 a9, v211\n\t"
    "v_accvgpr_read_b32 v211, a10\n\t"
    "v_mul_f32 v211, v211, v210\n\t"
    "v_accvgpr_write_b32 a10, v211\n\t"
    "v_accvgpr_read_b32 v211, a11\n\t"
    "v_mul_f32 v211, v211, v210\n\t"
    "v_accvgpr_write_b32 a11, v211\n\t"
    "v_accvgpr_read_b32 v211, a12\n\t"
    "v_mul_f32 v211, v211, v214\n\t"
    "v_accvgpr_write_b32 a12, v211\n\t"
    "v_accvgpr_read_b32 v211, a13\n\t"
    "v_mul_f32 v211, v211, v214\n\t"
    "v_accvgpr_write_b32 a13, v211\n\t"
    "v_accvgpr_read_b32 v211, a14\n\t"
    "v_mul_f32 v211, v211, v214\n\t"
    "v_accvgpr_write_b32 a14, v211\n\t"
    "v_accvgpr_read_b32 v211, a15\n\t"
    "v_mul_f32 v211, v211, v214\n\t"
    "v_accvgpr_write_b32 a15, v211\n\t"
    "v_accvgpr_read_b32 v211, a16\n\t"
    "v_mul_f32 v211, v211, v210\n\t"
    "v_accvgpr_write_b32 a16, v211\n\t"
    "v_accvgpr_read_b32 v211, a17\n\t"
    "v_mul_f32 v211, v211, v210\n\t"
    "v_accvgpr_write_b32 a17, v211\n\t"
    "v_accvgpr_read_b32 v211, a18\n\t"
    "v_mul_f32 v211, v211, v210\n\t"
    "v_accvgpr_write_b32 a18, v211\n\t"
    "v_accvgpr_read_b32 v211, a19\n\t"
    "v_mul_f32 v211, v211, v210\n\t"
    "v_accvgpr_write_b32 a19, v211\n\t"
    "v_accvgpr_read_b32 v211, a20\n\t"
    "v_mul_f32 v211, v211, v214\n\t"
    "v_accvgpr_write_b32 a20, v211\n\t"
    "v_accvgpr_read_b32 v211, a21\n\t"
    "v_mul_f32 v211, v211, v214\n\t"
    "v_accvgpr_write_b32 a21, v211\n\t"
    "v_accvgpr_read_b32 v211, a22\n\t"
    "v_mul_f32 v211, v211, v214\n\t"
    "v_accvgpr_write_b32 a22, v211\n\t"
    "v_accvgpr_read_b32 v211, a23\n\t"
    "v_mul_f32 v211, v211, v214\n\t"
    "v_accvgpr_write_b32 a23, v211\n\t"
    "v_accvgpr_read_b32 v211, a24\n\t"
    "v_mul_f32 v211, v211, v210\n\t"
    "v_accvgpr_write_b32 a24, v211\n\t"
    "v_accvgpr_read_b32 v211, a25\n\t"
    "v_mul_f32 v211, v211, v210\n\t"
    "v_accvgpr_write_b32 a25, v211\n\t"
    "v_accvgpr_read_b32 v211, a26\n\t"
    "v_mul_f32 v211, v211, v210\n\t"
    "v_accvgpr_write_b32 a26, v211\n\t"
    "v_accvgpr_read_b32 v211, a27\n\t"
    "v_mul_f32 v211, v211, v210\n\t"
    "v_accvgpr_write_b32 a27, v211\n\t"
    "v_accvgpr_read_b32 v211, a28\n\t"
    "v_mul_f32 v211, v211, v214\n\t"
    "v_accvgpr_write_b32 a28, v211\n\t"
    "v_accvgpr_read_b32 v211, a29\n\t"
    "v_mul_f32 v211, v211, v214\n\t"
    "v_accvgpr_write_b32 a29, v211\n\t"
    "v_accvgpr_read_b32 v211, a30\n\t"
    "v_mul_f32 v211, v211, v214\n\t"
    "v_accvgpr_write_b32 a30, v211\n\t"
    "v_accvgpr_read_b32 v211, a31\n\t"
    "v_mul_f32 v211, v211, v214\n\t"
    "v_accvgpr_write_b32 a31, v211\n\t"
    "v_accvgpr_read_b32 v211, a32\n\t"
    "v_mul_f32 v211, v211, v210\n\t"
    "v_accvgpr_write_b32 a32, v211\n\t"
    "v_accvgpr_read_b32 v211, a33\n\t"
    "v_mul_f32 v211, v211, v210\n\t"
    "v_accvgpr_write_b32 a33, v211\n\t"
    "v_accvgpr_read_b32 v211, a34\n\t"
    "v_mul_f32 v211, v211, v210\n\t"
    "v_accvgpr_write_b32 a34, v211\n\t"
    "v_accvgpr_read_b32 v211, a35\n\t"
    "v_mul_f32 v211, v211, v210\n\t"
    "v_accvgpr_write_b32 a35, v211\n\t"
    "v_accvgpr_read_b32 v211, a36\n\t"
    "v_mul_f32 v211, v211, v214\n\t"
    "v_accvgpr_write_b32 a36, v211\n\t"
    "v_accvgpr_read_b32 v211, a37\n\t"
    "v_mul_f32 v211, v211, v214\n\t"
    "v_accvgpr_write_b32 a37, v211\n\t"
    "v_accvgpr_read_b32 v211, a38\n\t"
    "v_mul_f32 v211, v211, v214\n\t"
    "v_accvgpr_write_b32 a38, v211\n\t"
    "v_accvgpr_read_b32 v211, a39\n\t"
    "v_mul_f32 v211, v211, v214\n\t"
    "v_accvgpr_write_b32 a39, v211\n\t"
    "v_accvgpr_read_b32 v211, a40\n\t"
    "v_mul_f32 v211, v211, v210\n\t"
    "v_accvgpr_write_b32 a40, v211\n\t"
    "v_accvgpr_read_b32 v211, a41\n\t"
    "v_mul_f32 v211, v211, v210\n\t"
    "v_accvgpr_write_b32 a41, v211\n\t"
    "v_accvgpr_read_b32 v211, a42\n\t"
    "v_mul_f32 v211, v211, v210\n\t"
    "v_accvgpr_write_b32 a42, v211\n\t"
    "v_accvgpr_read_b32 v211, a43\n\t"
    "v_mul_f32 v211, v211, v210\n\t"
    "v_accvgpr_write_b32 a43, v211\n\t"
    "v_accvgpr_read_b32 v211, a44\n\t"
    "v_mul_f32 v211, v211, v214\n\t"
    "v_accvgpr_write_b32 a44, v211\n\t"
    "v_accvgpr_read_b32 v211, a45\n\t"
    "v_mul_f32 v211, v211, v214\n\t"
    "v_accvgpr_write_b32 a45, v211\n\t"
    "v_accvgpr_read_b32 v211, a46\n\t"
    "v_mul_f32 v211, v211, v214\n\t"
    "v_accvgpr_write_b32 a46, v211\n\t"
    "v_accvgpr_read_b32 v211, a47\n\t"
    "v_mul_f32 v211, v211, v214\n\t"
    "v_accvgpr_write_b32 a47, v211\n\t"
    "v_accvgpr_read_b32 v211, a48\n\t"
    "v_mul_f32 v211, v211, v210\n\t"
    "v_accvgpr_write_b32 a48, v211\n\t"
    "v_accvgpr_read_b32 v211, a49\n\t"
    "v_mul_f32 v211, v211, v210\n\t"
    "v_accvgpr_write_b32 a49, v211\n\t"
    "v_accvgpr_read_b32 v211, a50\n\t"
    "v_mul_f32 v211, v211, v210\n\t"
    "v_accvgpr_write_b32 a50, v211\n\t"
    "v_accvgpr_read_b32 v211, a51\n\t"
    "v_mul_f32 v211, v211, v210\n\t"
    "v_accvgpr_write_b32 a51, v211\n\t"
    "v_accvgpr_read_b32 v211, a52\n\t"
    "v_mul_f32 v211, v211, v214\n\t"
    "v_accvgpr_write_b32 a52, v211\n\t"
    "v_accvgpr_read_b32 v211, a53\n\t"
    "v_mul_f32 v211, v211, v214\n\t"
    "v_accvgpr_write_b32 a53, v211\n\t"
    "v_accvgpr_read_b32 v211, a54\n\t"
    "v_mul_f32 v211, v211, v214\n\t"
    "v_accvgpr_write_b32 a54, v211\n\t"
    "v_accvgpr_read_b32 v211, a55\n\t"
    "v_mul_f32 v211, v211, v214\n\t"
    "v_accvgpr_write_b32 a55, v211\n\t"
    "v_accvgpr_read_b32 v211, a56\n\t"
    "v_mul_f32 v211, v211, v210\n\t"
    "v_accvgpr_write_b32 a56, v211\n\t"
    "v_accvgpr_read_b32 v211, a57\n\t"
    "v_mul_f32 v211, v211, v210\n\t"
    "v_accvgpr_write_b32 a57, v211\n\t"
    "v_accvgpr_read_b32 v211, a58\n\t"
    "v_mul_f32 v211, v211, v210\n\t"
    "v_accvgpr_write_b32 a58, v211\n\t"
    "v_accvgpr_read_b32 v211, a59\n\t"
    "v_mul_f32 v211, v211, v210\n\t"
    "v_accvgpr_write_b32 a59, v211\n\t"
    "v_accvgpr_read_b32 v211, a60\n\t"
    "v_mul_f32 v211, v211, v214\n\t"
    "v_accvgpr_write_b32 a60, v211\n\t"
    "v_accvgpr_read_b32 v211, a61\n\t"
    "v_mul_f32 v211, v211, v214\n\t"
    "v_accvgpr_write_b32 a61, v211\n\t"
    "v_accvgpr_read_b32 v211, a62\n\t"
    "v_mul_f32 v211, v211, v214\n\t"
    "v_accvgpr_write_b32 a62, v211\n\t"
    "v_accvgpr_read_b32 v211, a63\n\t"
    "v_mul_f32 v211, v211, v214\n\t"
    "v_accvgpr_write_b32 a63, v211\n\t"
    "v_sub_f32 v128, v128, v209\n\t"
    "v_sub_f32 v129, v129, v209\n\t"
    "v_sub_f32 v130, v130, v209\n\t"
    "v_sub_f32 v131, v131, v209\n\t"
    "v_sub_f32 v132, v132, v209\n\t"
    "v_sub_f32 v133, v133, v209\n\t"
    "v_sub_f32 v134, v134, v209\n\t"
    "v_sub_f32 v135, v135, v209\n\t"
    "v_sub_f32 v136, v136, v213\n\t"
    "v_sub_f32 v137, v137, v213\n\t"
    "v_sub_f32 v138, v138, v213\n\t"
    "v_sub_f32 v139, v139, v213\n\t"
    "v_sub_f32 v140, v140, v213\n\t"
    "v_sub_f32 v141, v141, v213\n\t"
    "v_sub_f32 v142, v142, v213\n\t"
    "v_sub_f32 v143, v143, v213\n\t"
    "v_exp_f32 v128, v128\n\t"
    "v_exp_f32 v129, v129\n\t"
    "v_exp_f32 v130, v130\n\t"
    "v_exp_f32 v131, v131\n\t"
    "v_exp_f32 v132, v132\n\t"
    "v_exp_f32 v133, v133\n\t"
    "v_exp_f32 v134, v134\n\t"
    "v_exp_f32 v135, v135\n\t"
    "v_exp_f32 v136, v136\n\t"
    "v_exp_f32 v137, v137\n\t"
    "v_exp_f32 v138, v138\n\t"
    "v_exp_f32 v139, v139\n\t"
    "v_exp_f32 v140, v140\n\t"
    "v_exp_f32 v141, v141\n\t"
    "v_exp_f32 v142, v142\n\t"
    "v_exp_f32 v143, v143\n\t"
    "s_nop 0\n\t"
    "v_cvt_pk_bf16_f32 v192, v128, v129\n\t"
    "v_cvt_pk_bf16_f32 v193, v130, v131\n\t"
    "v_cvt_pk_bf16_f32 v194, v132, v133\n\t"
    "v_cvt_pk_bf16_f32 v195, v134, v135\n\t"
    "v_cvt_pk_bf16_f32 v196, v136, v137\n\t"
    "v_cvt_pk_bf16_f32 v197, v138, v139\n\t"
    "v_cvt_pk_bf16_f32 v198, v140, v141\n\t"
    "v_cvt_pk_bf16_f32 v199, v142, v143\n\t"
    "s_nop 1\n\t"
    "v_mfma_f32_16x16x32_bf16 v[208:211], v[224:227], v[192:195], 0\n\t"
    "v_mfma_f32_16x16x32_bf16 v[212:215], v[224:227], v[196:199], 0\n\t"
    "s_nop 15\n\t"
    "s_setpc_b64 s[52:53]\n\t"
    ".Ltsv_rare10:\n\t"
    "s_nop 15\n\t"
    "v_max3_f32 v217, v144, v145, v146\n\t"
    "v_max3_f32 v217, v217, v147, v148\n\t"
    "v_max3_f32 v217, v217, v149, v150\n\t"
    "v_max_f32 v217, v217, v151\n\t"
    "v_max3_f32 v221, v152, v153, v154\n\t"
    "v_max3_f32 v221, v221, v155, v156\n\t"
    "v_max3_f32 v221, v221, v157, v158\n\t"
    "v_max_f32 v221, v221, v159\n\t"
    "s_nop 1\n\t"
    "v_permlane32_swap_b32 v217, v221\n\t"
    "v_max_f32 v217, v217, v221\n\t"
    "v_mov_b32 v221, v217\n\t"
    "s_nop 1\n\t"
    "v_permlane16_swap_b32 v217, v221\n\t"
    "v_max_f32 v217, v217, v221\n\t"
    "v_mov_b32 v221, v217\n\t"
    "s_nop 1\n\t"
    "v_permlane32_swap_b32 v217, v221\n\t"
    "v_max_f32 v217, 0, v217\n\t"
    "v_max_f32 v221, 0, v221\n\t"
    "v_exp_f32 v218, -v217\n\t"
    "v_exp_f32 v222, -v221\n\t"
    "v_sub_f32 v168, v168, v217\n\t"
    "v_sub_f32 v169, v169, v217\n\t"
    "v_sub_f32 v170, v170, v217\n\t"
    "v_sub_f32 v171, v171, v217\n\t"
    "v_mul_f32 v252, v252, v218\n\t"
    "v_sub_f32 v172, v172, v221\n\t"
    "v_sub_f32 v173, v173, v221\n\t"
    "v_sub_f32 v174, v174, v221\n\t"
    "v_sub_f32 v175, v175, v221\n\t"
    "v_mul_f32 v253, v253, v222\n\t"
    "v_mul_f32 v64, v64, v218\n\t"
    "v_mul_f32 v65, v65, v218\n\t"
    "v_mul_f32 v66, v66, v218\n\t"
    "v_mul_f32 v67, v67, v218\n\t"
    "v_mul_f32 v68, v68, v222\n\t"
    "v_mul_f32 v69, v69, v222\n\t"
    "v_mul_f32 v70, v70, v222\n\t"
    "v_mul_f32 v71, v71, v222\n\t"
    "v_mul_f32 v72, v72, v218\n\t"
    "v_mul_f32 v73, v73, v218\n\t"
    "v_mul_f32 v74, v74, v218\n\t"
    "v_mul_f32 v75, v75, v218\n\t"
    "v_mul_f32 v76, v76, v222\n\t"
    "v_mul_f32 v77, v77, v222\n\t"
    "v_mul_f32 v78, v78, v222\n\t"
    "v_mul_f32 v79, v79, v222\n\t"
    "v_mul_f32 v80, v80, v218\n\t"
    "v_mul_f32 v81, v81, v218\n\t"
    "v_mul_f32 v82, v82, v218\n\t"
    "v_mul_f32 v83, v83, v218\n\t"
    "v_mul_f32 v84, v84, v222\n\t"
    "v_mul_f32 v85, v85, v222\n\t"
    "v_mul_f32 v86, v86, v222\n\t"
    "v_mul_f32 v87, v87, v222\n\t"
    "v_mul_f32 v88, v88, v218\n\t"
    "v_mul_f32 v89, v89, v218\n\t"
    "v_mul_f32 v90, v90, v218\n\t"
    "v_mul_f32 v91, v91, v218\n\t"
    "v_mul_f32 v92, v92, v222\n\t"
    "v_mul_f32 v93, v93, v222\n\t"
    "v_mul_f32 v94, v94, v222\n\t"
    "v_mul_f32 v95, v95, v222\n\t"
    "v_mul_f32 v96, v96, v218\n\t"
    "v_mul_f32 v97, v97, v218\n\t"
    "v_mul_f32 v98, v98, v218\n\t"
    "v_mul_f32 v99, v99, v218\n\t"
    "v_mul_f32 v100, v100, v222\n\t"
    "v_mul_f32 v101, v101, v222\n\t"
    "v_mul_f32 v102, v102, v222\n\t"
    "v_mul_f32 v103, v103, v222\n\t"
    "v_mul_f32 v104, v104, v218\n\t"
    "v_mul_f32 v105, v105, v218\n\t"
    "v_mul_f32 v106, v106, v218\n\t"
    "v_mul_f32 v107, v107, v218\n\t"
    "v_mul_f32 v108, v108, v222\n\t"
    "v_mul_f32 v109, v109, v222\n\t"
    "v_mul_f32 v110, v110, v222\n\t"
    "v_mul_f32 v111, v111, v222\n\t"
    "v_mul_f32 v112, v112, v218\n\t"
    "v_mul_f32 v113, v113, v218\n\t"
    "v_mul_f32 v114, v114, v218\n\t"
    "v_mul_f32 v115, v115, v218\n\t"
    "v_mul_f32 v116, v116, v222\n\t"
    "v_mul_f32 v117, v117, v222\n\t"
    "v_mul_f32 v118, v118, v222\n\t"
    "v_mul_f32 v119, v119, v222\n\t"
    "v_mul_f32 v120, v120, v218\n\t"
    "v_mul_f32 v121, v121, v218\n\t"
    "v_mul_f32 v122, v122, v218\n\t"
    "v_mul_f32 v123, v123, v218\n\t"
    "v_mul_f32 v124, v124, v222\n\t"
    "v_mul_f32 v125, v125, v222\n\t"
    "v_mul_f32 v126, v126, v222\n\t"
    "v_mul_f32 v127, v127, v222\n\t"
    "v_sub_f32 v144, v144, v217\n\t"
    "v_sub_f32 v145, v145, v217\n\t"
    "v_sub_f32 v146, v146, v217\n\t"
    "v_sub_f32 v147, v147, v217\n\t"
    "v_sub_f32 v148, v148, v217\n\t"
    "v_sub_f32 v149, v149, v217\n\t"
    "v_sub_f32 v150, v150, v217\n\t"
    "v_sub_f32 v151, v151, v217\n\t"
    "v_sub_f32 v152, v152, v221\n\t"
    "v_sub_f32 v153, v153, v221\n\t"
    "v_sub_f32 v154, v154, v221\n\t"
    "v_sub_f32 v155, v155, v221\n\t"
    "v_sub_f32 v156, v156, v221\n\t"
    "v_sub_f32 v157, v157, v221\n\t"
    "v_sub_f32 v158, v158, v221\n\t"
    "v_sub_f32 v159, v159, v221\n\t"
    "v_exp_f32 v144, v144\n\t"
    "v_exp_f32 v145, v145\n\t"
    "v_exp_f32 v146, v146\n\t"
    "v_exp_f32 v147, v147\n\t"
    "v_exp_f32 v148, v148\n\t"
    "v_exp_f32 v149, v149\n\t"
    "v_exp_f32 v150, v150\n\t"
    "v_exp_f32 v151, v151\n\t"
    "v_exp_f32 v152, v152\n\t"
    "v_exp_f32 v153, v153\n\t"
    "v_exp_f32 v154, v154\n\t"
    "v_exp_f32 v155, v155\n\t"
    "v_exp_f32 v156, v156\n\t"
    "v_exp_f32 v157, v157\n\t"
    "v_exp_f32 v158, v158\n\t"
    "v_exp_f32 v159, v159\n\t"
    "s_nop 0\n\t"
    "v_cvt_pk_bf16_f32 v184, v144, v145\n\t"
    "v_cvt_pk_bf16_f32 v185, v146, v147\n\t"
    "v_cvt_pk_bf16_f32 v186, v148, v149\n\t"
    "v_cvt_pk_bf16_f32 v187, v150, v151\n\t"
    "v_cvt_pk_bf16_f32 v188, v152, v153\n\t"
    "v_cvt_pk_bf16_f32 v189, v154, v155\n\t"
    "v_cvt_pk_bf16_f32 v190, v156, v157\n\t"
    "v_cvt_pk_bf16_f32 v191, v158, v159\n\t"
    "s_nop 1\n\t"
    "v_mfma_f32_16x16x32_bf16 v[216:219], v[224:227], v[184:187], 0\n\t"
    "v_mfma_f32_16x16x32_bf16 v[220:223], v[224:227], v[188:191], 0\n\t"
    "s_nop 15\n\t"
    "s_setpc_b64 s[52:53]\n\t"
    ".Ltsv_rare11:\n\t"
    "s_nop 15\n\t"
    "v_max3_f32 v217, v144, v145, v146\n\t"
    "v_max3_f32 v217, v217, v147, v148\n\t"
    "v_max3_f32 v217, v217, v149, v150\n\t"
    "v_max_f32 v217, v217, v151\n\t"
    "v_max3_f32 v221, v152, v153, v154\n\t"
    "v_max3_f32 v221, v221, v155, v156\n\t"
    "v_max3_f32 v221, v221, v157, v158\n\t"
    "v_max_f32 v221, v221, v159\n\t"
    "s_nop 1\n\t"
    "v_permlane32_swap_b32 v217, v221\n\t"
    "v_max_f32 v217, v217, v221\n\t"
    "v_mov_b32 v221, v217\n\t"
    "s_nop 1\n\t"
    "v_permlane16_swap_b32 v217, v221\n\t"
    "v_max_f32 v217, v217, v221\n\t"
    "v_mov_b32 v221, v217\n\t"
    "s_nop 1\n\t"
    "v_permlane32_swap_b32 v217, v221\n\t"
    "v_max_f32 v217, 0, v217\n\t"
    "v_max_f32 v221, 0, v221\n\t"
    "v_exp_f32 v218, -v217\n\t"
    "v_exp_f32 v222, -v221\n\t"
    "v_sub_f32 v168, v168, v217\n\t"
    "v_sub_f32 v169, v169, v217\n\t"
    "v_sub_f32 v170, v170, v217\n\t"
    "v_sub_f32 v171, v171, v217\n\t"
    "v_mul_f32 v252, v252, v218\n\t"
    "v_sub_f32 v172, v172, v221\n\t"
    "v_sub_f32 v173, v173, v221\n\t"
    "v_sub_f32 v174, v174, v221\n\t"
    "v_sub_f32 v175, v175, v221\n\t"
    "v_mul_f32 v253, v253, v222\n\t"
    "v_mul_f32 v64, v64, v218\n\t"
    "v_mul_f32 v65, v65, v218\n\t"
    "v_mul_f32 v66, v66, v218\n\t"
    "v_mul_f32 v67, v67, v218\n\t"
    "v_mul_f32 v68, v68, v222\n\t"
    "v_mul_f32 v69, v69, v222\n\t"
    "v_mul_f32 v70, v70, v222\n\t"
    "v_mul_f32 v71, v71, v222\n\t"
    "v_mul_f32 v72, v72, v218\n\t"
    "v_mul_f32 v73, v73, v218\n\t"
    "v_mul_f32 v74, v74, v218\n\t"
    "v_mul_f32 v75, v75, v218\n\t"
    "v_mul_f32 v76, v76, v222\n\t"
    "v_mul_f32 v77, v77, v222\n\t"
    "v_mul_f32 v78, v78, v222\n\t"
    "v_mul_f32 v79, v79, v222\n\t"
    "v_mul_f32 v80, v80, v218\n\t"
    "v_mul_f32 v81, v81, v218\n\t"
    "v_mul_f32 v82, v82, v218\n\t"
    "v_mul_f32 v83, v83, v218\n\t"
    "v_mul_f32 v84, v84, v222\n\t"
    "v_mul_f32 v85, v85, v222\n\t"
    "v_mul_f32 v86, v86, v222\n\t"
    "v_mul_f32 v87, v87, v222\n\t"
    "v_mul_f32 v88, v88, v218\n\t"
    "v_mul_f32 v89, v89, v218\n\t"
    "v_mul_f32 v90, v90, v218\n\t"
    "v_mul_f32 v91, v91, v218\n\t"
    "v_mul_f32 v92, v92, v222\n\t"
    "v_mul_f32 v93, v93, v222\n\t"
    "v_mul_f32 v94, v94, v222\n\t"
    "v_mul_f32 v95, v95, v222\n\t"
    "v_mul_f32 v96, v96, v218\n\t"
    "v_mul_f32 v97, v97, v218\n\t"
    "v_mul_f32 v98, v98, v218\n\t"
    "v_mul_f32 v99, v99, v218\n\t"
    "v_mul_f32 v100, v100, v222\n\t"
    "v_mul_f32 v101, v101, v222\n\t"
    "v_mul_f32 v102, v102, v222\n\t"
    "v_mul_f32 v103, v103, v222\n\t"
    "v_mul_f32 v104, v104, v218\n\t"
    "v_mul_f32 v105, v105, v218\n\t"
    "v_mul_f32 v106, v106, v218\n\t"
    "v_mul_f32 v107, v107, v218\n\t"
    "v_mul_f32 v108, v108, v222\n\t"
    "v_mul_f32 v109, v109, v222\n\t"
    "v_mul_f32 v110, v110, v222\n\t"
    "v_mul_f32 v111, v111, v222\n\t"
    "v_mul_f32 v112, v112, v218\n\t"
    "v_mul_f32 v113, v113, v218\n\t"
    "v_mul_f32 v114, v114, v218\n\t"
    "v_mul_f32 v115, v115, v218\n\t"
    "v_mul_f32 v116, v116, v222\n\t"
    "v_mul_f32 v117, v117, v222\n\t"
    "v_mul_f32 v118, v118, v222\n\t"
    "v_mul_f32 v119, v119, v222\n\t"
    "v_mul_f32 v120, v120, v218\n\t"
    "v_mul_f32 v121, v121, v218\n\t"
    "v_mul_f32 v122, v122, v218\n\t"
    "v_mul_f32 v123, v123, v218\n\t"
    "v_mul_f32 v124, v124, v222\n\t"
    "v_mul_f32 v125, v125, v222\n\t"
    "v_mul_f32 v126, v126, v222\n\t"
    "v_mul_f32 v127, v127, v222\n\t"
    "v_sub_f32 v144, v144, v217\n\t"
    "v_sub_f32 v145, v145, v217\n\t"
    "v_sub_f32 v146, v146, v217\n\t"
    "v_sub_f32 v147, v147, v217\n\t"
    "v_sub_f32 v148, v148, v217\n\t"
    "v_sub_f32 v149, v149, v217\n\t"
    "v_sub_f32 v150, v150, v217\n\t"
    "v_sub_f32 v151, v151, v217\n\t"
    "v_sub_f32 v152, v152, v221\n\t"
    "v_sub_f32 v153, v153, v221\n\t"
    "v_sub_f32 v154, v154, v221\n\t"
    "v_sub_f32 v155, v155, v221\n\t"
    "v_sub_f32 v156, v156, v221\n\t"
    "v_sub_f32 v157, v157, v221\n\t"
    "v_sub_f32 v158, v158, v221\n\t"
    "v_sub_f32 v159, v159, v221\n\t"
    "v_exp_f32 v144, v144\n\t"
    "v_exp_f32 v145, v145\n\t"
    "v_exp_f32 v146, v146\n\t"
    "v_exp_f32 v147, v147\n\t"
    "v_exp_f32 v148, v148\n\t"
    "v_exp_f32 v149, v149\n\t"
    "v_exp_f32 v150, v150\n\t"
    "v_exp_f32 v151, v151\n\t"
    "v_exp_f32 v152, v152\n\t"
    "v_exp_f32 v153, v153\n\t"
    "v_exp_f32 v154, v154\n\t"
    "v_exp_f32 v155, v155\n\t"
    "v_exp_f32 v156, v156\n\t"
    "v_exp_f32 v157, v157\n\t"
    "v_exp_f32 v158, v158\n\t"
    "v_exp_f32 v159, v159\n\t"
    "s_nop 0\n\t"
    "v_cvt_pk_bf16_f32 v200, v144, v145\n\t"
    "v_cvt_pk_bf16_f32 v201, v146, v147\n\t"
    "v_cvt_pk_bf16_f32 v202, v148, v149\n\t"
    "v_cvt_pk_bf16_f32 v203, v150, v151\n\t"
    "v_cvt_pk_bf16_f32 v204, v152, v153\n\t"
    "v_cvt_pk_bf16_f32 v205, v154, v155\n\t"
    "v_cvt_pk_bf16_f32 v206, v156, v157\n\t"
    "v_cvt_pk_bf16_f32 v207, v158, v159\n\t"
    "s_nop 1\n\t"
    "v_mfma_f32_16x16x32_bf16 v[216:219], v[224:227], v[200:203], 0\n\t"
    "v_mfma_f32_16x16x32_bf16 v[220:223], v[224:227], v[204:207], 0\n\t"
    "s_nop 15\n\t"
    "s_setpc_b64 s[52:53]\n\t"
    ".Ltsv_end:\n\t"
    : [tid] "+v"(tid)
    : [ka] "s"(__builtin_amdgcn_kernarg_segment_ptr()), [bid] "s"(blockIdx.x), [ldsb] "s"((unsigned)(size_t)lds)
    : "v1", "v2", "v3", "v4", "v5", "v6", "v7", "v8", "v9", "v10", "v11", "v12", "v13", "v14", "v15", "v16", "v17", "v18", "v19", "v20", "v21", "v22", "v23", "v24", "v25", "v26", "v27", "v28", "v29", "v30", "v31", "v32", "v33", "v34", "v35", "v36", "v37", "v38", "v39", "v40", "v41", "v42", "v43", "v44", "v45", "v46", "v47", "v48", "v49", "v50", "v51", "v52", "v53", "v54", "v55", "v56", "v57", "v58", "v59", "v60", "v61", "v62", "v63", "v64", "v65", "v66", "v67", "v68", "v69", "v70", "v71", "v72", "v73", "v74", "v75", "v76", "v77", "v78", "v79", "v80", "v81", "v82", "v83", "v84", "v85", "v86", "v87", "v88", "v89", "v90", "v91", "v92", "v93", "v94", "v95", "v96", "v97", "v98", "v99", "v100", "v101", "v102", "v103", "v104", "v105", "v106", "v107", "v108", "v109", "v110", "v111", "v112", "v113", "v114", "v115", "v116", "v117", "v118", "v119", "v120", "v121", "v122", "v123", "v124", "v125", "v126", "v127", "v128", "v129", "v130", "v131", "v132", "v133", "v134", "v135", "v136", "v137", "v138", "v139", "v140", "v141", "v142", "v143", "v144", "v145", "v146", "v147", "v148", "v149", "v150", "v151", "v152", "v153", "v154", "v155", "v156", "v157", "v158", "v159", "v160", "v161", "v162", "v163", "v164", "v165", "v166", "v167", "v168", "v169", "v170", "v171", "v172", "v173", "v174", "v175", "v176", "v177", "v178", "v179", "v180", "v181", "v182", "v183", "v184", "v185", "v186", "v187", "v188", "v189", "v190", "v191", "v192", "v193", "v194", "v195", "v196", "v197", "v198", "v199", "v200", "v201", "v202", "v203", "v204", "v205", "v206", "v207", "v208", "v209", "v210", "v211", "v212", "v213", "v214", "v215", "v216", "v217", "v218", "v219", "v220", "v221", "v222", "v223", "v224", "v225", "v226", "v227", "v228", "v229", "v230", "v231", "v232", "v233", "v234", "v235", "v236", "v237", "v238", "v239", "v240", "v241", "v242", "v243", "v244", "v245", "v246", "v247", "v248", "v249", "v250", "v251", "v252", "v253", "v254", "v255", "a0", "a1", "a2", "a3", "a4", "a5", "a6", "a7", "a8", "a9", "a10", "a11", "a12", "a13", "a14", "a15", "a16", "a17", "a18", "a19", "a20", "a21", "a22", "a23", "a24", "a25", "a26", "a27", "a28", "a29", "a30", "a31", "a32", "a33", "a34", "a35", "a36", "a37", "a38", "a39", "a40", "a41", "a42", "a43", "a44", "a45", "a46", "a47", "a48", "a49", "a50", "a51", "a52", "a53", "a54", "a55", "a56", "a57", "a58", "a59", "a60", "a61", "a62", "a63", "a64", "a65", "a66", "a67", "a68", "a69", "a70", "a71", "a72", "a73", "a74", "a75", "a76", "a77", "a78", "a79", "a80", "a81", "a82", "a83", "a84", "a85", "a86", "a87", "a88", "a89", "a90", "a91", "a92", "a93", "a94", "a95", "a96", "a97", "a98", "a99", "a100", "a101", "a102", "a103", "a104", "a105", "a106", "a107", "a108", "a109", "a110", "a111", "a112", "a113", "a114", "a115", "a116", "a117", "a118", "a119", "a120", "a121", "a122", "a123", "a124", "a125", "a126", "a127", "a128", "a129", "a130", "a131", "a132", "a133", "a134", "a135", "a136", "a137", "a138", "a139", "a140", "a141", "a142", "a143", "a144", "a145", "a146", "a147", "a148", "a149", "a150", "a151", "a152", "a153", "a154", "a155", "a156", "a157", "a158", "a159", "s8", "s9", "s10", "s11", "s12", "s13", "s14", "s15", "s16", "s17", "s18", "s19", "s20", "s21", "s22", "s23", "s24", "s25", "s26", "s27", "s28", "s29", "s30", "s31", "s32", "s33", "s34", "s35", "s36", "s37", "s38", "s39", "s40", "s41", "s42", "s43", "s44", "s45", "s46", "s47", "s48", "s49", "s50", "s51", "s52", "s53", "s54", "s55", "s56", "s57", "s58", "s59", "s60", "s61", "s62", "s63", "s64", "s65", "s66", "s67", "s68", "s69", "s70", "s71", "s72", "s73", "s74", "s75", "s76", "s77", "s78", "s79", "s80", "s81", "s82", "s83", "s84", "s85", "s86", "s87", "s88", "s89", "s90", "s91", "s92", "s93", "s94", "s95", "s96", "s97", "s98", "s99", "vcc", "scc", "memory");
}

extern "C" void kernel_launch(void* const* d_in, const int* in_sizes, int n_in, void* d_out, int out_size, void* d_ws, size_t ws_size, hipStream_t stream) {
  tsv_attn_kernel<<<256, 256, 0, stream>>>((const char*)d_in[0], (const char*)d_in[1], (const char*)d_in[2], (const char*)d_in[3], (const char*)d_in[4],
                                          (const char*)d_in[5], (char*)d_out, (char*)d_ws);
}
```
